```python
import torch

def _softmax(x, axis=-1):
    return torch.softmax(x, dim=axis)
def _gelu(x):
    return torch.nn.functional.gelu(x, approximate="tanh")
def _silu(x):
    return torch.nn.functional.silu(x)
def _sigmoid(x):
    return torch.sigmoid(x)
def _layernorm(x, axis=-1, eps=1e-5):
    m = x.mean(dim=axis, keepdim=True)
    v = x.var(dim=axis, keepdim=True, unbiased=False)
    return (x - m) / torch.sqrt(v + eps)
def _scatter_add(tgt, idx, src, axis=0):
    return tgt.clone().index_add_(axis, idx, src)
def _segment_sum(src, seg, n):
    out = torch.zeros((n,) + src.shape[1:], dtype=src.dtype)
    return out.index_add_(0, seg, src)
def _pad(x, pw):
    flat = tuple(v for pair in reversed(pw) for v in pair)
    return torch.nn.functional.pad(x, flat)

kernel_name = 'sequential_preLN__grouped_kv_mqa__moe_arch_mod__prompt'

def setup_inputs():

    _w = (lambda a: (lambda t: t.to(torch.bfloat16) if t.is_floating_point() else t)(torch.as_tensor(a)))
    def _A_setup():
        B, S, D, H = 1, 2048, 640, 32
        Dh = D // H
        return {
            "x": _w((lambda *s: torch.randn(*s, dtype=torch.bfloat16) * 0.05)(B, S, D)),
            "wq": _w((lambda *s: torch.randn(*s, dtype=torch.bfloat16) * 0.05)(D, H * Dh)),
            "wk": _w((lambda *s: torch.randn(*s, dtype=torch.bfloat16) * 0.05)(D, Dh)),
            "wv": _w((lambda *s: torch.randn(*s, dtype=torch.bfloat16) * 0.05)(D, Dh)),
            "wo": _w((lambda *s: torch.randn(*s, dtype=torch.bfloat16) * 0.05)(H * Dh, D)),
        }
    _A_in = _A_setup()

    def _B_setup():
        B, S, D, F, C = 1, 2048, 640, 1728, 1
        return {
            "x": _w((lambda *s: torch.randn(*s, dtype=torch.bfloat16) * 0.05)(B, S, D)),
            "w_r": _w((lambda *s: torch.randn(*s, dtype=torch.bfloat16) * 0.05)(D)),
            "w_gate": _w((lambda *s: torch.randn(*s, dtype=torch.bfloat16) * 0.05)(D, F)),
            "w_up": _w((lambda *s: torch.randn(*s, dtype=torch.bfloat16) * 0.05)(D, F)),
            "w_down": _w((lambda *s: torch.randn(*s, dtype=torch.bfloat16) * 0.05)(F, D)),
        }
    _B_in = _B_setup()

    return {
        "A_x": _A_in["x"],
        "A_wq": _A_in["wq"],
        "A_wk": _A_in["wk"],
        "A_wv": _A_in["wv"],
        "A_wo": _A_in["wo"],
        "B_x": _B_in["x"],
        "B_w_r": _B_in["w_r"],
        "B_w_gate": _B_in["w_gate"],
        "B_w_up": _B_in["w_up"],
        "B_w_down": _B_in["w_down"],
    }

def reference(A_x, A_wq, A_wk, A_wv, A_wo, B_x, B_w_r, B_w_gate, B_w_up, B_w_down):
    def _A_ref(x, wq, wk, wv, wo):
        B, S, D = x.shape
        H = 32
        Dh = D // H
        q = (lambda x, *d: x.permute(*d))((x @ wq).reshape(B, S, H, Dh), 0, 2, 1, 3)
        k = x @ wk
        v = x @ wv
        scores = torch.einsum("bhqd,bkd->bhqk", q, k) * (Dh ** -0.5)
        p = _softmax(scores, axis=-1)
        o = torch.einsum("bhqk,bkd->bhqd", p, v)
        return (lambda x, *d: x.permute(*d))(o, 0, 2, 1, 3).reshape(B, S, H * Dh) @ wo

    def _B_ref(x, w_r, w_gate, w_up, w_down):
        B, S, D = x.shape
        C = 1
        r = x @ w_r
        idx = torch.argsort(-r, axis=-1)[:, :C]
        idx_e = (lambda x, n, axis: torch.repeat_interleave(x, n, dim=axis))(idx.reshape(B, C, 1), D, axis=2)
        x_sel = (lambda x, i, axis: torch.take_along_dim(x, i, dim=axis))(x, idx_e, axis=1)
        g = _silu(x_sel @ w_gate)
        u = x_sel @ w_up
        y = (g * u) @ w_down
        r_sel = _sigmoid((lambda x, i, axis: torch.take_along_dim(x, i, dim=axis))(r, idx, axis=1))
        y = y * r_sel.reshape(B, C, 1)
        b_off = (torch.arange(B) * S).reshape(B, 1)
        flat_idx = (idx + b_off).reshape(B * C)
        zero = (x * 0.0).reshape(B * S, D)
        upd = _scatter_add(zero, flat_idx, y.reshape(B * C, D), axis=0)
        return x + upd.reshape(B, S, D)

    x = A_x
    h = x + _A_ref(_layernorm(x, axis=-1), A_wq, A_wk, A_wv, A_wo)
    h2 = h + _B_ref(_layernorm(h, axis=-1), B_w_r, B_w_gate, B_w_up, B_w_down)
    return h2
```

```cpp
#include <hip/hip_runtime.h>
#include <hip/hip_bf16.h>
#include <stdint.h>

typedef unsigned short u16;
using bf16x8 = __attribute__((ext_vector_type(8))) short;
using bf16x4 = __attribute__((ext_vector_type(4))) short;
using f32x16 = __attribute__((ext_vector_type(16))) float;
using f32x4 = __attribute__((ext_vector_type(4))) float;
using f32x2 = __attribute__((ext_vector_type(2))) float;
using bfx2 = __attribute__((ext_vector_type(2))) __bf16;
using u32x4 = __attribute__((ext_vector_type(4))) unsigned;
using u32x2 = __attribute__((ext_vector_type(2))) unsigned;
#define LAS __attribute__((address_space(3)))
typedef LAS const char* lds_cptr;
typedef LAS char* lds_ptr;
#define SBAR() __builtin_amdgcn_sched_barrier(0)
#define PIN(x) asm volatile("" : "+v"(x))
#define MFMA16(a, b, c) __builtin_amdgcn_mfma_f32_32x32x16_bf16(a, b, c, 0, 0, 0)
#define MFMA8(a, b, c) __builtin_amdgcn_mfma_f32_32x32x8bf16_1k(a, b, c, 0, 0, 0)
#define MFMAS(a, b, c) __builtin_amdgcn_mfma_f32_16x16x32_bf16(a, b, c, 0, 0, 0)
#define DI __device__ __forceinline__

constexpr int S = 2048, D = 640, H = 32, DH = 20, FF = 1728;
constexpr int TILE_B = 2560, CHUNK_T = 16, CHUNK_B = TILE_B * CHUNK_T;
constexpr float QSCALE = 0.22360679774997896f * 1.4426950408889634f;
constexpr float LN_EPS = 1e-5f;

constexpr size_t WS_Q = 0;
constexpr size_t WS_O = WS_Q + (size_t)S * D * 2;
constexpr size_t WS_H = WS_O + (size_t)S * D * 2;
constexpr size_t WS_KV = WS_H + (size_t)S * D * 2;
constexpr size_t WS_KMAX = WS_KV + 64 * TILE_B;
constexpr size_t WS_STAT = WS_KMAX + 512;
constexpr size_t WS_BEST = WS_STAT + (size_t)S * 8 * 16;
constexpr size_t WS_ACT = WS_BEST + 256;
constexpr size_t WS_ROWST = WS_ACT + 8192;

DI unsigned cvtpk(float lo, float hi) { return __builtin_bit_cast(unsigned, __builtin_convertvector((f32x2){lo, hi}, bfx2)); }
DI float bflo(unsigned w) { return __uint_as_float(w << 16); }
DI float bfhi(unsigned w) { return __uint_as_float(w & 0xffff0000u); }
DI float bfr(float v) { return bflo(cvtpk(v, 0.f)); }
DI void glds16(const void* g, unsigned lds_base) {
  unsigned sv;
  asm volatile("s_mov_b32 %0, m0\n\ts_mov_b32 m0, %2\n\ts_nop 0\n\tglobal_load_lds_dwordx4 %1, off\n\ts_mov_b32 m0, %0" : "=&s"(sv) : "v"(g), "s"(lds_base) : "memory");
}
DI bf16x4 trread(lds_cptr p) { return __builtin_bit_cast(bf16x4, __builtin_amdgcn_ds_read_tr16_b64_v4i16((LAS bf16x4*)p)); }
DI bf16x8 cat8(bf16x4 a, bf16x4 b) { return __builtin_shufflevector(a, b, 0, 1, 2, 3, 4, 5, 6, 7); }
DI float dot2(unsigned a, unsigned b, float c) { return __builtin_amdgcn_fdot2_f32_bf16(__builtin_bit_cast(bfx2, a), __builtin_bit_cast(bfx2, b), c, false); }

#define GLOAD16(dst, ptr, off) asm volatile("global_load_dwordx4 %0, %1, off offset:" #off : "=v"(dst) : "v"(ptr))

constexpr int L1_WQ = 0, L1_WKV = 102400, L1_ST = 128064, L1_CS = L1_ST + 1024, L1_KVX = L1_CS + 896, L1_QX = L1_KVX + 2048, L1_END = L1_QX + 20480;

__global__ __launch_bounds__(512, 2) void k_qkv(const u16* __restrict__ x, const u16* __restrict__ wq, const u16* __restrict__ wk, const u16* __restrict__ wv,
                                                char* __restrict__ ws) {
  extern __shared__ __attribute__((aligned(16))) char lds[];
  const int tid = threadIdx.x, lane = tid & 63, c = lane & 15, g = lane >> 4;
  const int wid = __builtin_amdgcn_readfirstlane(tid >> 6);
  const int mt = wid & 3, kh = wid >> 2;
  const int b = blockIdx.x, xcd = b & 7, slot = b >> 3, rg = xcd * 4 + (slot >> 3), hg = slot & 7;
  const int kvmt = hg >> 1, isV = hg & 1, rank = (mt - kvmt - 1) & 3;
  const unsigned lds0 = (unsigned)(uintptr_t)lds;
  if (b == 0 && tid == 0) *(unsigned long long*)(ws + WS_BEST) = 0ull;

  u32x4 xf[10];
  {
    const char* xp = (const char*)x + (size_t)(rg * 64 + mt * 16 + c) * 1280 + kh * 640 + g * 16;
    GLOAD16(xf[0], xp, 0); GLOAD16(xf[1], xp, 64); GLOAD16(xf[2], xp, 128); GLOAD16(xf[3], xp, 192); GLOAD16(xf[4], xp, 256);
    GLOAD16(xf[5], xp, 320); GLOAD16(xf[6], xp, 384); GLOAD16(xf[7], xp, 448); GLOAD16(xf[8], xp, 512); GLOAD16(xf[9], xp, 576);
  }
  {
    const char* src = (const char*)(isV ? wv : wk);
#pragma unroll
    for (int j = 0; j < 4; ++j) {
      const int i = wid + 8 * j;
      if (i < 25) glds16(src + i * 1024 + lane * 16, (unsigned)__builtin_amdgcn_readfirstlane(lds0 + L1_WKV + i * 1024));
    }
  }
#pragma unroll
  for (int j = 0; j < 13; ++j) {
    const int idx = wid + 8 * j;
    if (idx < 100) {
      const int i = (idx & 1) * 50 + (idx >> 1);
      const int ci = i * 64 + lane, row = ci / 10, cc = ci - row * 10;
      glds16((const char*)wq + (size_t)row * 1280 + hg * 160 + cc * 16, (unsigned)__builtin_amdgcn_readfirstlane(lds0 + L1_WQ + i * 1024));
    }
  }
  asm volatile("s_waitcnt vmcnt(12)\n\ts_barrier"
               : "+v"(xf[0]), "+v"(xf[1]), "+v"(xf[2]), "+v"(xf[3]), "+v"(xf[4]), "+v"(xf[5]), "+v"(xf[6]), "+v"(xf[7]), "+v"(xf[8]), "+v"(xf[9])
               :
               : "memory");
  if (g & 1) {
#pragma unroll
    for (int s = 0; s < 10; ++s) xf[s] = (u32x4){xf[s][2], xf[s][3], xf[s][0], xf[s][1]};
  }
  const int q4 = c >> 2, p4 = c & 3;
  const int row1 = 320 * kh + 8 * g + (g & 1) * 4 + q4, row2 = 320 * kh + 8 * g + (1 - (g & 1)) * 4 + q4;
  const bf16x8 ones = __builtin_bit_cast(bf16x8, (u32x4){0x3f803f80u, 0x3f803f80u, 0x3f803f80u, 0x3f803f80u});
  f32x4 ax[3] = {};
  if (rank == 3 || rank == 0) {
    lds_cptr k1 = (lds_cptr)lds + L1_WKV + row1 * 40 + p4 * 8, k2 = (lds_cptr)lds + L1_WKV + row2 * 40 + p4 * 8;
#pragma unroll
    for (int s = 0; s < 10; ++s) {
#pragma unroll
      for (int u = 0; u < 2; ++u) {
        bf16x8 wf = cat8(trread(k1 + s * 1280 + u * 32), trread(k2 + s * 1280 + u * 32));
        bf16x8 xs = __builtin_bit_cast(bf16x8, xf[s]);
        if (rank == 0) ax[u] = MFMAS(wf, ones, ax[u]);
        else if (isV) ax[u] = MFMAS(xs, wf, ax[u]);
        else ax[u] = MFMAS(wf, xs, ax[u]);
      }
    }
  }
  {
    float s1 = 0.f, s2 = 0.f;
#pragma unroll
    for (int s = 0; s < 10; ++s)
#pragma unroll
      for (int j = 0; j < 4; ++j) { s1 = dot2(xf[s][j], 0x3f803f80u, s1); s2 = dot2(xf[s][j], xf[s][j], s2); }
    s1 += __shfl_xor(s1, 16); s2 += __shfl_xor(s2, 16);
    s1 += __shfl_xor(s1, 32); s2 += __shfl_xor(s2, 32);
    if (g == 0) *(f32x2*)(lds + L1_ST + (wid * 16 + c) * 8) = (f32x2){s1, s2};
  }
  float* csp = (float*)(lds + L1_CS);
  if (rank == 0) {
    if (c == 0) { *(f32x4*)(csp + (kh * 7 + 5) * 16 + 4 * g) = ax[0]; *(f32x4*)(csp + (kh * 7 + 6) * 16 + 4 * g) = ax[1]; }
    ax[0] = (f32x4){}; ax[1] = (f32x4){};
  }
  if (rank == 3 && kh == 1) {
    *(f32x4*)(lds + L1_KVX + lane * 16) = ax[0]; *(f32x4*)(lds + L1_KVX + 1024 + lane * 16) = ax[1];
  }
  asm volatile("s_waitcnt vmcnt(0) lgkmcnt(0)\n\ts_barrier" ::: "memory");
  auto row_stats = [&](int m, int p, float& mean, float& rstd) {
    f32x2 a = *(const f32x2*)(lds + L1_ST + (m * 16 + p) * 8), bb = *(const f32x2*)(lds + L1_ST + ((m + 4) * 16 + p) * 8);
    mean = (a[0] + bb[0]) * (1.0f / D);
    float var = (a[1] + bb[1]) * (1.0f / D) - mean * mean;
    var = fmaxf(var, 0.f);
    rstd = 1.0f / sqrtf(var + LN_EPS);
  };
  if (rank == 3 && kh == 0) {
    f32x4 a0 = ax[0] + *(const f32x4*)(lds + L1_KVX + lane * 16), a1 = ax[1] + *(const f32x4*)(lds + L1_KVX + 1024 + lane * 16);
    const int gm = rg * 4 + kvmt;
    char* tb = ws + WS_KV + (size_t)(gm >> 1) * TILE_B;
    if (!isV) {
      float mean, rstd; row_stats(mt, c, mean, rstd);
      f32x4 c0 = *(const f32x4*)(csp + 5 * 16 + 4 * g) + *(const f32x4*)(csp + (7 + 5) * 16 + 4 * g);
      f32x4 c1 = *(const f32x4*)(csp + 6 * 16 + 4 * g) + *(const f32x4*)(csp + (7 + 6) * 16 + 4 * g);
      unsigned w0 = cvtpk((a0[0] - mean * c0[0]) * rstd, (a0[1] - mean * c0[1]) * rstd), w1 = cvtpk((a0[2] - mean * c0[2]) * rstd, (a0[3] - mean * c0[3]) * rstd);
      unsigned w2 = cvtpk((a1[0] - mean * c1[0]) * rstd, (a1[1] - mean * c1[1]) * rstd), w3 = cvtpk((a1[2] - mean * c1[2]) * rstd, (a1[3] - mean * c1[3]) * rstd);
      const int r = (gm & 1) * 16 + c;
      *(u32x2*)(tb + ((g >> 1) * 32 + r) * 16 + (g & 1) * 8) = (u32x2){w0, w1};
      float n2 = dot2(w0, w0, dot2(w1, w1, 0.f));
      if (g == 0) { *(u32x2*)(tb + 1024 + r * 8) = (u32x2){w2, w3}; n2 = dot2(w2, w2, dot2(w3, w3, n2)); }
      n2 += __shfl_xor(n2, 16); n2 += __shfl_xor(n2, 32);
      n2 = fmaxf(n2, __shfl_xor(n2, 1)); n2 = fmaxf(n2, __shfl_xor(n2, 2)); n2 = fmaxf(n2, __shfl_xor(n2, 4)); n2 = fmaxf(n2, __shfl_xor(n2, 8));
      if (lane == 0) ((float*)(ws + WS_KMAX))[gm] = n2;
    } else {
      float cs0 = csp[5 * 16 + c] + csp[(7 + 5) * 16 + c], cs1 = csp[6 * 16 + c] + csp[(7 + 6) * 16 + c];
      float v0[4], v1[4];
#pragma unroll
      for (int i = 0; i < 4; ++i) {
        float mean, rstd; row_stats(mt, 4 * g + i, mean, rstd);
        v0[i] = (a0[i] - mean * cs0) * rstd; v1[i] = (a1[i] - mean * cs1) * rstd;
      }
      const int s = gm & 1;
      char* vb = tb + 1280 + ((s * 2 + (g & 1)) * 20) * 16 + (g >> 1) * 8;
      *(u32x2*)(vb + c * 16) = (u32x2){cvtpk(v0[0], v0[1]), cvtpk(v0[2], v0[3])};
      if (c < 4) *(u32x2*)(vb + (16 + c) * 16) = (u32x2){cvtpk(v1[0], v1[1]), cvtpk(v1[2], v1[3])};
    }
  }
  f32x4 acc[5] = {};
  {
    lds_cptr w1 = (lds_cptr)lds + L1_WQ + row1 * 160 + p4 * 8, w2 = (lds_cptr)lds + L1_WQ + row2 * 160 + p4 * 8;
#pragma unroll
    for (int s = 0; s < 10; ++s) {
      bf16x8 xs = __builtin_bit_cast(bf16x8, xf[s]);
      bf16x8 wf[5];
#pragma unroll
      for (int t = 0; t < 5; ++t) wf[t] = cat8(trread(w1 + s * 5120 + t * 32), trread(w2 + s * 5120 + t * 32));
#pragma unroll
      for (int t = 0; t < 5; ++t) acc[t] = MFMAS(wf[t], xs, acc[t]);
      if (rank == 0) ax[2] = MFMAS(wf[0], ones, ax[2]);
      else if (rank == 1) { ax[0] = MFMAS(wf[1], ones, ax[0]); ax[1] = MFMAS(wf[2], ones, ax[1]); }
      else if (rank == 2) { ax[0] = MFMAS(wf[3], ones, ax[0]); ax[1] = MFMAS(wf[4], ones, ax[1]); }
    }
  }
  if (c == 0) {
    if (rank == 0) *(f32x4*)(csp + (kh * 7 + 0) * 16 + 4 * g) = ax[2];
    else if (rank == 1) { *(f32x4*)(csp + (kh * 7 + 1) * 16 + 4 * g) = ax[0]; *(f32x4*)(csp + (kh * 7 + 2) * 16 + 4 * g) = ax[1]; }
    else if (rank == 2) { *(f32x4*)(csp + (kh * 7 + 3) * 16 + 4 * g) = ax[0]; *(f32x4*)(csp + (kh * 7 + 4) * 16 + 4 * g) = ax[1]; }
  }
  char* qx = lds + L1_QX + mt * 5120;
  if (kh == 0) { *(f32x4*)(qx + 3 * 1024 + lane * 16) = acc[3]; *(f32x4*)(qx + 4 * 1024 + lane * 16) = acc[4]; }
  else { *(f32x4*)(qx + 0 * 1024 + lane * 16) = acc[0]; *(f32x4*)(qx + 1 * 1024 + lane * 16) = acc[1]; *(f32x4*)(qx + 2 * 1024 + lane * 16) = acc[2]; }
  __syncthreads();
  {
    float mean, rstd; row_stats(mt, c, mean, rstd);
    rstd *= QSCALE;
    u16* qo = (u16*)(ws + WS_Q) + (size_t)(rg * 64 + mt * 16 + c) * D + hg * 80 + 4 * g;
    auto fin = [&](int t, f32x4 a) {
      a += *(const f32x4*)(qx + t * 1024 + lane * 16);
      f32x4 cs = *(const f32x4*)(csp + t * 16 + 4 * g) + *(const f32x4*)(csp + (7 + t) * 16 + 4 * g);
      *(u32x2*)(qo + t * 16) = (u32x2){cvtpk((a[0] - mean * cs[0]) * rstd, (a[1] - mean * cs[1]) * rstd), cvtpk((a[2] - mean * cs[2]) * rstd, (a[3] - mean * cs[3]) * rstd)};
    };
    if (kh == 0) { fin(0, acc[0]); fin(1, acc[1]); fin(2, acc[2]); }
    else { fin(3, acc[3]); fin(4, acc[4]); }
  }
}

constexpr int L2_BYTES = 3 * CHUNK_B + 64;
struct QF { bf16x8 lo; bf16x4 hi; };
DI void set_bias(QF& f, int h, float m) {
  float nm = -m;
  float a = bfr(nm), r1 = nm - a, bb = bfr(r1), cc = r1 - bb;
  if (h == 1) f.hi = __builtin_bit_cast(bf16x4, (u32x2){cvtpk(a, bb), cvtpk(cc, 0.f)});
}
DI QF load_q(const u16* qp, int h, float km2) {
  u32x2 qa = *(const u32x2*)(qp + 8 * h), qb = *(const u32x2*)(qp + 8 * h + 4), qc = *(const u32x2*)(qp + 16);
  float n2 = dot2(qa[0], qa[0], dot2(qa[1], qa[1], dot2(qb[0], qb[0], dot2(qb[1], qb[1], 0.f))));
  if (h == 0) n2 = dot2(qc[0], qc[0], dot2(qc[1], qc[1], n2));
  n2 += __shfl_xor(n2, 32);
  QF f;
  f.lo = __builtin_bit_cast(bf16x8, (u32x4){qa[0], qa[1], qb[0], qb[1]});
  f.hi = __builtin_bit_cast(bf16x4, qc);
  set_bias(f, h, sqrtf(n2 * km2) * 1.0009765625f);
  return f;
}

__global__ __launch_bounds__(512, 2) void k_attn(char* __restrict__ ws) {
  extern __shared__ __attribute__((aligned(16))) char lds[];
  const int tid = threadIdx.x, lane = tid & 63, r = lane & 31, h = lane >> 5;
  const int wid = __builtin_amdgcn_readfirstlane(tid >> 6);
  const int b = blockIdx.x, xcd = b & 7, slot = b >> 3, rg = xcd * 4 + (slot >> 3), hg = slot & 7;
  const unsigned lds0 = (unsigned)(uintptr_t)lds;
  const char* kvimg = ws + WS_KV;
  auto dma_chunk = [&](int c, int buf) {
#pragma unroll
    for (int i = 0; i < 5; ++i)
      glds16(kvimg + (size_t)c * CHUNK_B + (i * 8 + wid) * 1024 + lane * 16, (unsigned)__builtin_amdgcn_readfirstlane(lds0 + buf * CHUNK_B + (i * 8 + wid) * 1024));
  };
  float km2;
  {
    const float* kp = (const float*)(ws + WS_KMAX);
    km2 = fmaxf(kp[lane], kp[lane + 64]);
#pragma unroll
    for (int o = 1; o < 64; o <<= 1) km2 = fmaxf(km2, __shfl_xor(km2, o));
  }
  const int kh = wid >> 2;
  const int pos = rg * 64 + r, head = hg * 4 + (wid & 3);
  const u16* q = (const u16*)(ws + WS_Q);
  QF qa = load_q(q + (size_t)pos * D + head * DH, h, km2);
  QF qb = load_q(q + (size_t)(pos + 32) * D + head * DH, h, km2);
  const unsigned oKA = lane * 16, oKB = 1024 + r * 8, oV = 1280 + (h * 20 + r) * 16;
  const bool vact = r < 20, kact = h == 0;
  volatile int* flag = (volatile int*)(lds + 3 * CHUNK_B);
  if (tid == 0) *flag = 0;
  f32x16 F;
#pragma unroll 1
  for (int pass = 0; pass < 2; ++pass) {
    dma_chunk(0, 0); dma_chunk(1, 1); dma_chunk(2, 2);
    f32x16 O = {}, O2 = {}, SA, SB;
    bf16x8 klo, v0, v1; bf16x4 khi;
    u32x4 p0, p1 = {0, 0, 0, 0};
    {
      unsigned cv = (r == 20) ? 0x3f803f80u : 0u;
      v0 = __builtin_bit_cast(bf16x8, (u32x4){cv, cv, cv, cv}); v1 = v0;
      khi = __builtin_bit_cast(bf16x4, (u32x2){0x3f803f80u, 0x00003f80u});
    }
#define LDK(base, t) do { klo = *(const LAS bf16x8*)((base) + oKA + (t) * TILE_B); if (kact) khi = *(const LAS bf16x4*)((base) + oKB + (t) * TILE_B); } while (0)
#define LDV0(base, t) do { if (vact) v0 = *(const LAS bf16x8*)((base) + oV + (t) * TILE_B); } while (0)
#define LDV1(base, t) do { if (vact) v1 = *(const LAS bf16x8*)((base) + oV + 640 + (t) * TILE_B); } while (0)
#define EXG(X, i, P, j) do { X[i] = __builtin_amdgcn_exp2f(X[i]); X[i + 1] = __builtin_amdgcn_exp2f(X[i + 1]); P[j] = cvtpk(X[i], X[i + 1]); \
                             X[i + 2] = __builtin_amdgcn_exp2f(X[i + 2]); X[i + 3] = __builtin_amdgcn_exp2f(X[i + 3]); P[j + 1] = cvtpk(X[i + 2], X[i + 3]); PIN(P); SBAR(); } while (0)
#define AS8(p) __builtin_bit_cast(bf16x8, p)
#define STEPA(j) do { \
    O2 = MFMA16(v1, AS8(p1), O2); SBAR(); \
    LDV1(cb, 2 * (j)); SBAR(); \
    EXG(SA, 0, p0, 0); \
    SB = MFMA16(klo, qb.lo, (f32x16){}); SBAR(); \
    EXG(SA, 4, p0, 2); \
    SB = MFMA8(khi, qb.hi, SB); SBAR(); \
    if ((j) + 1 < 8) LDK(cb, 2 * ((j) + 1)); else LDK(nb, 0); SBAR(); \
    EXG(SA, 8, p1, 0); \
    O = MFMA16(v0, AS8(p0), O); SBAR(); \
    EXG(SA, 12, p1, 2); \
  } while (0)
#define STEPB(j) do { \
    O = MFMA16(v1, AS8(p1), O); SBAR(); \
    EXG(SB, 0, p0, 0); \
    SA = MFMA16(klo, qa.lo, (f32x16){}); SBAR(); \
    EXG(SB, 4, p0, 2); \
    SA = MFMA8(khi, qa.hi, SA); SBAR(); \
    EXG(SB, 8, p1, 0); \
    O2 = MFMA16(v0, AS8(p0), O2); SBAR(); \
    if ((j) + 1 < 8) LDV0(cb, 2 * ((j) + 1)); else LDV0(nb, 0); SBAR(); \
    EXG(SB, 12, p1, 2); \
  } while (0)
    asm volatile("s_waitcnt vmcnt(0) lgkmcnt(0)\n\ts_barrier" ::: "memory");
    lds_cptr cb = (lds_cptr)lds + kh * TILE_B, nb = cb + CHUNK_B;
    LDK(cb, 0);
    SA = MFMA16(klo, qa.lo, (f32x16){});
    SA = MFMA8(khi, qa.hi, SA);
    LDV0(cb, 0);
    SBAR();
#pragma unroll 1
    for (int c = 0; c < 4; ++c) {
      STEPA(0); STEPB(0); STEPA(1); STEPB(1); STEPA(2); STEPB(2); STEPA(3); STEPB(3);
      STEPA(4); STEPB(4); STEPA(5); STEPB(5); STEPA(6); STEPB(6);
      if (c == 1) { asm volatile("s_barrier" ::: "memory"); dma_chunk(3, 0); }
      if (c == 2) { asm volatile("s_waitcnt vmcnt(0)\n\ts_barrier" ::: "memory"); }
      STEPA(7); STEPB(7);
      cb = nb; nb = (c == 1) ? (lds_cptr)lds + kh * TILE_B : nb + CHUNK_B;
      if (c == 2) nb = (lds_cptr)lds + kh * TILE_B + CHUNK_B;
    }
    O2 = MFMA16(v1, AS8(p1), O2);
    asm volatile("s_waitcnt lgkmcnt(0)\n\ts_barrier" ::: "memory");
    {
      float* ex = (float*)(lds + CHUNK_B) + wid * 12 * 64 + lane;
      if (kh == 0) {
#pragma unroll
        for (int i = 0; i < 12; ++i) ex[i * 64] = O2[i];
        F = O;
      } else {
#pragma unroll
        for (int i = 0; i < 12; ++i) ex[i * 64] = O[i];
        F = O2;
      }
    }
    __syncthreads();
    {
      const float* rx = (const float*)(lds + CHUNK_B) + (wid ^ 4) * 12 * 64 + lane;
#pragma unroll
      for (int i = 0; i < 12; ++i) F[i] += rx[i * 64];
    }
    if (pass == 1) break;
    const bool bad = (h == 1) && !(F[8] >= 5.421010862427522e-20f);
    if (__any(bad) && lane == 0) *flag = 1;
    __syncthreads();
    if (*flag == 0) break;
    {
      QF za = qa, zb = qb;
      if (h == 1) { za.hi = (bf16x4){}; zb.hi = (bf16x4){}; }
      float ma = -INFINITY, mb = -INFINITY;
#pragma unroll 1
      for (int t = 0; t < 64; ++t) {
        const char* tb = kvimg + (size_t)t * TILE_B;
        bf16x8 kl = *(const bf16x8*)(tb + oKA);
        bf16x4 kk = *(const bf16x4*)(tb + oKB);
        f32x16 sa = MFMA8(kk, za.hi, MFMA16(kl, za.lo, (f32x16){}));
        f32x16 sb = MFMA8(kk, zb.hi, MFMA16(kl, zb.lo, (f32x16){}));
#pragma unroll
        for (int i = 0; i < 16; ++i) { ma = fmaxf(ma, sa[i]); mb = fmaxf(mb, sb[i]); }
      }
      ma = fmaxf(ma, __shfl_xor(ma, 32)); mb = fmaxf(mb, __shfl_xor(mb, 32));
      set_bias(qa, h, ma); set_bias(qb, h, mb);
    }
    __syncthreads();
  }
  {
    u16* op = (u16*)(ws + WS_O) + (size_t)(pos + 32 * kh) * D + head * DH;
    float l = __shfl(F[8], r + 32);
    float rl = 1.0f / l;
    *(u32x2*)(op + 4 * h) = (u32x2){cvtpk(F[0] * rl, F[1] * rl), cvtpk(F[2] * rl, F[3] * rl)};
    *(u32x2*)(op + 8 + 4 * h) = (u32x2){cvtpk(F[4] * rl, F[5] * rl), cvtpk(F[6] * rl, F[7] * rl)};
    if (h == 0) *(u32x2*)(op + 16) = (u32x2){cvtpk(F[8] * rl, F[9] * rl), cvtpk(F[10] * rl, F[11] * rl)};
  }
}

constexpr int L3_WO = 0, L3_QX = 102400, L3_ST = L3_QX + 20480, L3_END = L3_ST + 8 * 16 * 16;
__global__ __launch_bounds__(512, 2) void k_oproj(const u16* __restrict__ x, const u16* __restrict__ wo, const u16* __restrict__ wr, char* __restrict__ ws) {
  extern __shared__ __attribute__((aligned(16))) char lds[];
  const int tid = threadIdx.x, lane = tid & 63, c = lane & 15, g = lane >> 4;
  const int wid = __builtin_amdgcn_readfirstlane(tid >> 6);
  const int mt = wid & 3, kh = wid >> 2;
  const int b = blockIdx.x, xcd = b & 7, slot = b >> 3, rg = xcd * 4 + (slot >> 3), hg = slot & 7;
  const unsigned lds0 = (unsigned)(uintptr_t)lds;
  const int row = rg * 64 + mt * 16 + c;
  u32x4 xf[10];
  {
    const char* xp = ws + WS_O + (size_t)row * 1280 + kh * 640 + g * 16;
    GLOAD16(xf[0], xp, 0); GLOAD16(xf[1], xp, 64); GLOAD16(xf[2], xp, 128); GLOAD16(xf[3], xp, 192); GLOAD16(xf[4], xp, 256);
    GLOAD16(xf[5], xp, 320); GLOAD16(xf[6], xp, 384); GLOAD16(xf[7], xp, 448); GLOAD16(xf[8], xp, 512); GLOAD16(xf[9], xp, 576);
  }
#pragma unroll
  for (int j = 0; j < 13; ++j) {
    const int idx = wid + 8 * j;
    if (idx < 100) {
      const int i = (idx & 1) * 50 + (idx >> 1);
      const int ci = i * 64 + lane, rw = ci / 10, cc = ci - rw * 10;
      glds16((const char*)wo + (size_t)rw * 1280 + hg * 160 + cc * 16, (unsigned)__builtin_amdgcn_readfirstlane(lds0 + L3_WO + i * 1024));
    }
  }
  asm volatile("s_waitcnt vmcnt(0)\n\ts_barrier"
               : "+v"(xf[0]), "+v"(xf[1]), "+v"(xf[2]), "+v"(xf[3]), "+v"(xf[4]), "+v"(xf[5]), "+v"(xf[6]), "+v"(xf[7]), "+v"(xf[8]), "+v"(xf[9])
               :
               : "memory");
  if (g & 1) {
#pragma unroll
    for (int s = 0; s < 10; ++s) xf[s] = (u32x4){xf[s][2], xf[s][3], xf[s][0], xf[s][1]};
  }
  const int q4 = c >> 2, p4 = c & 3;
  const int row1 = 320 * kh + 8 * g + (g & 1) * 4 + q4, row2 = 320 * kh + 8 * g + (1 - (g & 1)) * 4 + q4;
  f32x4 acc[5] = {};
  {
    lds_cptr w1 = (lds_cptr)lds + L3_WO + row1 * 160 + p4 * 8, w2 = (lds_cptr)lds + L3_WO + row2 * 160 + p4 * 8;
#pragma unroll
    for (int s = 0; s < 10; ++s) {
      bf16x8 xs = __builtin_bit_cast(bf16x8, xf[s]);
      bf16x8 wf[5];
#pragma unroll
      for (int t = 0; t < 5; ++t) wf[t] = cat8(trread(w1 + s * 5120 + t * 32), trread(w2 + s * 5120 + t * 32));
#pragma unroll
      for (int t = 0; t < 5; ++t) acc[t] = MFMAS(wf[t], xs, acc[t]);
    }
  }
  char* qx = lds + L3_QX + mt * 5120;
  if (kh == 0) { *(f32x4*)(qx + 3 * 1024 + lane * 16) = acc[3]; *(f32x4*)(qx + 4 * 1024 + lane * 16) = acc[4]; }
  else { *(f32x4*)(qx + 0 * 1024 + lane * 16) = acc[0]; *(f32x4*)(qx + 1 * 1024 + lane * 16) = acc[1]; *(f32x4*)(qx + 2 * 1024 + lane * 16) = acc[2]; }
  __syncthreads();
  const int t0 = kh == 0 ? 0 : 3, nt = kh == 0 ? 3 : 2;
  float hv[12];
  float s1 = 0.f, dt = 0.f;
#pragma unroll
  for (int tt = 0; tt < 3; ++tt) {
    if (tt < nt) {
      const int t = t0 + tt;
      f32x4 a = (kh == 0 ? acc[tt] : acc[tt + 3 > 4 ? 4 : tt + 3]) + *(const f32x4*)(qx + t * 1024 + lane * 16);
      const int col = hg * 80 + t * 16 + 4 * g;
      u32x2 xv = *(const u32x2*)(x + (size_t)row * D + col);
      u32x2 wv = *(const u32x2*)(wr + col);
      unsigned a01 = cvtpk(a[0], a[1]), a23 = cvtpk(a[2], a[3]);
      unsigned h01 = cvtpk(bflo(xv[0]) + bflo(a01), bfhi(xv[0]) + bfhi(a01)), h23 = cvtpk(bflo(xv[1]) + bflo(a23), bfhi(xv[1]) + bfhi(a23));
      *(u32x2*)((u16*)(ws + WS_H) + (size_t)row * D + col) = (u32x2){h01, h23};
      hv[tt * 4 + 0] = bflo(h01); hv[tt * 4 + 1] = bfhi(h01); hv[tt * 4 + 2] = bflo(h23); hv[tt * 4 + 3] = bfhi(h23);
      s1 += (hv[tt * 4] + hv[tt * 4 + 1]) + (hv[tt * 4 + 2] + hv[tt * 4 + 3]);
      dt = dot2(h01, wv[0], dot2(h23, wv[1], dt));
    }
  }
  s1 += __shfl_xor(s1, 16); s1 += __shfl_xor(s1, 32);
  dt += __shfl_xor(dt, 16); dt += __shfl_xor(dt, 32);
  const float cnt = (float)(nt * 16);
  const float ml = s1 / cnt;
  float m2 = 0.f;
#pragma unroll
  for (int i = 0; i < 12; ++i) if (i < nt * 4) { float d = hv[i] - ml; m2 += d * d; }
  m2 += __shfl_xor(m2, 16); m2 += __shfl_xor(m2, 32);
  f32x4* st = (f32x4*)(lds + L3_ST);
  if (g == 0) st[wid * 16 + c] = (f32x4){s1, m2, dt, ml};
  __syncthreads();
  if (kh == 0 && g == 0) {
    f32x4 a = st[mt * 16 + c], bb = st[(mt + 4) * 16 + c];
    float mean = (a[0] + bb[0]) * (1.0f / 80.f);
    float da = a[3] - mean, db = bb[3] - mean;
    float M2 = a[1] + bb[1] + 48.f * da * da + 32.f * db * db;
    ((f32x4*)(ws + WS_STAT))[(size_t)row * 8 + hg] = (f32x4){mean, M2, a[2] + bb[2], 0.f};
  }
}

__global__ __launch_bounds__(256) void k_ln2(const u16* __restrict__ wr, char* __restrict__ ws, u16* __restrict__ out) {
  const int tid = threadIdx.x, lane = tid & 63, w = tid >> 6;
  float sw = 0.f;
  for (int i = lane; i < 320; i += 64) { unsigned v = ((const unsigned*)wr)[i]; sw += bflo(v) + bfhi(v); }
#pragma unroll
  for (int o = 1; o < 64; o <<= 1) sw += __shfl_xor(sw, o);
  unsigned long long best = 0;
  for (int rr = 0; rr < 2; ++rr) {
    const int row = blockIdx.x * 8 + w * 2 + rr;
    const f32x4* sp = (const f32x4*)(ws + WS_STAT) + (size_t)row * 8;
    float mean = 0.f, M2 = 0.f, dt = 0.f;
    f32x4 p[8];
#pragma unroll
    for (int i = 0; i < 8; ++i) { p[i] = sp[i]; mean += p[i][0]; dt += p[i][2]; }
    mean *= 0.125f;
#pragma unroll
    for (int i = 0; i < 8; ++i) { float d = p[i][0] - mean; M2 += p[i][1] + 80.f * d * d; }
    const float rstd = 1.0f / sqrtf(M2 * (1.0f / D) + LN_EPS);
    const float rv = rstd * (dt - mean * sw);
    if (lane == 0) { ((f32x2*)(ws + WS_ROWST))[row] = (f32x2){mean, rstd}; }
    unsigned ub = __float_as_uint(rv); ub = (ub & 0x80000000u) ? ~ub : (ub | 0x80000000u);
    unsigned long long key = ((unsigned long long)ub << 32) | (unsigned)(0xffffffffu - row);
    best = key > best ? key : best;
    const unsigned* hp = (const unsigned*)(ws + WS_H) + (size_t)row * 320;
    unsigned* op = (unsigned*)out + (size_t)row * 320;
    for (int i = lane; i < 320; i += 64) {
      unsigned hv = hp[i];
      float a = bflo(hv), bb = bfhi(hv);
      unsigned hn = cvtpk((a - mean) * rstd, (bb - mean) * rstd);
      op[i] = cvtpk(a + bflo(hn), bb + bfhi(hn));
    }
  }
  if (lane == 0) atomicMax((unsigned long long*)(ws + WS_BEST), best);
}

__global__ __launch_bounds__(256) void k_gateup(const u16* __restrict__ wg, const u16* __restrict__ wu, char* __restrict__ ws) {
  __shared__ float xs[D];
  __shared__ float red[2][64][33];
  const int tid = threadIdx.x;
  const unsigned long long best = *(const unsigned long long*)(ws + WS_BEST);
  const int row = (int)(0xffffffffu - (unsigned)best);
  const f32x2 st = ((const f32x2*)(ws + WS_ROWST))[row];
  for (int i = tid; i < D; i += 256) { float hv = bflo(((const u16*)(ws + WS_H))[(size_t)row * D + i]); xs[i] = bfr((hv - st[0]) * st[1]); }
  __syncthreads();
  const int cg = tid & 3, ks = tid >> 2;
  const int col = blockIdx.x * 32 + cg * 8;
  float ag[8] = {}, au[8] = {};
#pragma unroll
  for (int i = 0; i < 10; ++i) {
    const int k = ks * 10 + i;
    u32x4 a = *(const u32x4*)(wg + (size_t)k * FF + col), bb = *(const u32x4*)(wu + (size_t)k * FF + col);
    const float xv = xs[k];
#pragma unroll
    for (int j = 0; j < 4; ++j) {
      ag[2 * j] += xv * bflo(a[j]); ag[2 * j + 1] += xv * bfhi(a[j]);
      au[2 * j] += xv * bflo(bb[j]); au[2 * j + 1] += xv * bfhi(bb[j]);
    }
  }
#pragma unroll
  for (int j = 0; j < 8; ++j) { red[0][ks][cg * 8 + j] = ag[j]; red[1][ks][cg * 8 + j] = au[j]; }
  __syncthreads();
  if (tid < 32) {
    float gs = 0.f, us = 0.f;
    for (int i = 0; i < 64; ++i) { gs += red[0][i][tid]; us += red[1][i][tid]; }
    gs = bfr(gs); us = bfr(us);
    float sg = bfr(gs / (1.0f + __expf(-gs)));
    ((float*)(ws + WS_ACT))[blockIdx.x * 32 + tid] = bfr(sg * us);
  }
}

__global__ __launch_bounds__(256) void k_down(const u16* __restrict__ wd, char* __restrict__ ws, u16* __restrict__ out) {
  __shared__ float red[64][33];
  const int tid = threadIdx.x;
  const unsigned long long best = *(const unsigned long long*)(ws + WS_BEST);
  const int row = (int)(0xffffffffu - (unsigned)best);
  unsigned ub = (unsigned)(best >> 32); ub = (ub & 0x80000000u) ? (ub & 0x7fffffffu) : ~ub;
  const float rv = bfr(__uint_as_float(ub));
  const float* act = (const float*)(ws + WS_ACT);
  const int cg = tid & 3, ks = tid >> 2;
  const int col = blockIdx.x * 32 + cg * 8;
  float ay[8] = {};
#pragma unroll 9
  for (int i = 0; i < 27; ++i) {
    const int k = ks * 27 + i;
    u32x4 a = *(const u32x4*)(wd + (size_t)k * D + col);
    const float xv = act[k];
#pragma unroll
    for (int j = 0; j < 4; ++j) { ay[2 * j] += xv * bflo(a[j]); ay[2 * j + 1] += xv * bfhi(a[j]); }
  }
#pragma unroll
  for (int j = 0; j < 8; ++j) red[ks][cg * 8 + j] = ay[j];
  __syncthreads();
  if (tid < 32) {
    float y = 0.f;
    for (int i = 0; i < 64; ++i) y += red[i][tid];
    y = bfr(y);
    const float sg = bfr(1.0f / (1.0f + __expf(-rv)));
    y = bfr(y * sg);
    const int cc = blockIdx.x * 32 + tid;
    const f32x2 st = ((const f32x2*)(ws + WS_ROWST))[row];
    const float hv = bflo(((const u16*)(ws + WS_H))[(size_t)row * D + cc]);
    const float hn = bfr((hv - st[0]) * st[1]);
    out[(size_t)row * D + cc] = (u16)(cvtpk(hv + bfr(hn + y), 0.f) & 0xffffu);
  }
}

extern "C" void kernel_launch(void* const* d_in, const int* in_sizes, int n_in, void* d_out, int out_size, void* d_ws, size_t ws_size, hipStream_t stream) {
  const u16* x = (const u16*)d_in[0];
  const u16* wq = (const u16*)d_in[1];
  const u16* wk = (const u16*)d_in[2];
  const u16* wv = (const u16*)d_in[3];
  const u16* wo = (const u16*)d_in[4];
  const u16* wr = (const u16*)d_in[6];
  const u16* wg = (const u16*)d_in[7];
  const u16* wu = (const u16*)d_in[8];
  const u16* wd = (const u16*)d_in[9];
  char* ws = (char*)d_ws;
  u16* out = (u16*)d_out;
  hipFuncSetAttribute((const void*)k_qkv, hipFuncAttributeMaxDynamicSharedMemorySize, L1_END);
  hipFuncSetAttribute((const void*)k_attn, hipFuncAttributeMaxDynamicSharedMemorySize, L2_BYTES);
  hipFuncSetAttribute((const void*)k_oproj, hipFuncAttributeMaxDynamicSharedMemorySize, L3_END);
  k_qkv<<<256, 512, L1_END, stream>>>(x, wq, wk, wv, ws);
  k_attn<<<256, 512, L2_BYTES, stream>>>(ws);
  k_oproj<<<256, 512, L3_END, stream>>>(x, wo, wr, ws);
  k_ln2<<<256, 256, 0, stream>>>(wr, ws, out);
  k_gateup<<<54, 256, 0, stream>>>(wg, wu, ws);
  k_down<<<20, 256, 0, stream>>>(wd, ws, out);
}
```

```cpp
#include <hip/hip_runtime.h>
#include <hip/hip_bf16.h>
#include <stdint.h>

typedef unsigned short u16;
typedef unsigned long long u64;
using bf16x8 = __attribute__((ext_vector_type(8))) short;
using bf16x4 = __attribute__((ext_vector_type(4))) short;
using f32x16 = __attribute__((ext_vector_type(16))) float;
using f32x4 = __attribute__((ext_vector_type(4))) float;
using f32x2 = __attribute__((ext_vector_type(2))) float;
using bfx2 = __attribute__((ext_vector_type(2))) __bf16;
using u32x4 = __attribute__((ext_vector_type(4))) unsigned;
using u32x2 = __attribute__((ext_vector_type(2))) unsigned;
#define LAS __attribute__((address_space(3)))
#define GAS __attribute__((address_space(1)))
typedef LAS const char* lds_cptr;
typedef GAS unsigned gu32;
typedef GAS u64 gu64;
#define RLX_AGENT __ATOMIC_RELAXED, __HIP_MEMORY_SCOPE_AGENT
#define SBAR() __builtin_amdgcn_sched_barrier(0)
#define PIN(x) asm volatile("" : "+v"(x))
#define MFMA16(a, b, c) __builtin_amdgcn_mfma_f32_32x32x16_bf16(a, b, c, 0, 0, 0)
#define MFMA8(a, b, c) __builtin_amdgcn_mfma_f32_32x32x8bf16_1k(a, b, c, 0, 0, 0)
#define MFMAS(a, b, c) __builtin_amdgcn_mfma_f32_16x16x32_bf16(a, b, c, 0, 0, 0)
#define DI __device__ __forceinline__
#define AS8(p) __builtin_bit_cast(bf16x8, p)
#define AS4(p) __builtin_bit_cast(bf16x4, p)

constexpr int S = 2048, D = 640, H = 32, DH = 20, FF = 1728;
constexpr int TILE_B = 2624;
constexpr float QSCALE = 0.22360679774997896f * 1.4426950408889634f;
constexpr float LN_EPS = 1e-5f;
constexpr unsigned SPIN_CAP = 1u << 18;

constexpr size_t WS_EPOCH = 0;
constexpr size_t WS_F1 = 4096;
constexpr size_t WS_F2 = WS_F1 + 4096;
constexpr size_t WS_CAND = WS_F2 + 4096;
constexpr size_t WS_ACT = WS_CAND + 4096;
constexpr size_t WS_ROW = WS_ACT + 8192;
constexpr size_t WS_ST = WS_ROW + 32 * 5120;
constexpr size_t WS_KV = WS_ST + 256 * 1536;
constexpr size_t WS_O = WS_KV + 64 * TILE_B;
constexpr size_t WS_END = WS_O + (size_t)32 * 81920;

constexpr int L_X = 0, L_WKV = 81920, L_WQA = L_WKV + 25664, L_SC = L_WQA + 51200;
constexpr int L_KST = L_SC;
constexpr int L_KCS = L_KST + 256;
constexpr int L_KVX = L_KCS + 256;
constexpr int L_CNT = L_KVX + 2048;
constexpr int L_ST1 = L_WKV, L_CS = L_ST1 + 1024, L_MISC = L_CS + 640;
constexpr int L_WQB = 0, L_QX = 51200;
constexpr int L_WOA = 0;
constexpr int L_FGU = 51200;
constexpr int L_FD = L_FGU + 20480;
constexpr int L_OA = 99328;
constexpr int L_DUM = 148480;
constexpr int L_REDO = 150528;
constexpr int L_Q = 151552;
constexpr int L_QB = L_Q + 8 * 1280;
constexpr int L_WOB = 99328;
constexpr int LDS_BYTES = 163840;
constexpr int L_EX = 0;
constexpr int L_ST2 = L_EX + 20480;
constexpr int L_GS = L_ST2 + 2048;
constexpr int L_RED = L_GS + 6144;

struct Params {
  const u16 *x, *wq, *wk, *wv, *wo, *wr, *wg, *wu, *wd;
  u16* out;
  char* ws;
};

DI unsigned cvtpk(float lo, float hi) { return __builtin_bit_cast(unsigned, __builtin_convertvector((f32x2){lo, hi}, bfx2)); }
DI float bflo(unsigned w) { return __uint_as_float(w << 16); }
DI float bfhi(unsigned w) { return __uint_as_float(w & 0xffff0000u); }
DI float bfr(float v) { return bflo(cvtpk(v, 0.f)); }
DI void glds16(const void* g, unsigned lds_base) {
  unsigned sv;
  asm volatile("s_mov_b32 %0, m0\n\ts_mov_b32 m0, %2\n\ts_nop 0\n\tglobal_load_lds_dwordx4 %1, off\n\ts_mov_b32 m0, %0" : "=&s"(sv) : "v"(g), "s"(lds_base) : "memory");
}
DI bf16x4 trread(lds_cptr p) { return AS4(__builtin_amdgcn_ds_read_tr16_b64_v4i16((LAS bf16x4*)p)); }
DI bf16x8 cat8(bf16x4 a, bf16x4 b) { return __builtin_shufflevector(a, b, 0, 1, 2, 3, 4, 5, 6, 7); }
DI float dot2(unsigned a, unsigned b, float c) { return __builtin_amdgcn_fdot2_f32_bf16(__builtin_bit_cast(bfx2, a), __builtin_bit_cast(bfx2, b), c, false); }
DI void st_g64(char* p, unsigned lo, unsigned hi) { __hip_atomic_store((gu64*)p, ((u64)hi << 32) | lo, RLX_AGENT); }
DI u64 ld_g64(const char* p) { return __hip_atomic_load((gu64*)p, RLX_AGENT); }
#define GLOAD16(dst, ptr) asm volatile("global_load_dwordx4 %0, %1, off" : "=v"(dst) : "v"(ptr))
#define BAR_LGKM() asm volatile("s_waitcnt lgkmcnt(0)\n\ts_barrier" ::: "memory")
#define DRAIN() asm volatile("s_waitcnt vmcnt(0)" ::: "memory")

#define STAMP(i)
#define STAMP_DECL
#define STAMP_OUT()

__global__ __launch_bounds__(512, 2) void fused_kernel(Params p) {
  extern __shared__ __attribute__((aligned(16))) char lds[];
  char* ws = p.ws;
  const int tid = threadIdx.x, lane = tid & 63;
  const int wid = __builtin_amdgcn_readfirstlane(tid >> 6);
  const int c = lane & 15, g = lane >> 4;
  const int r = lane & 31, h = lane >> 5;
  const int mt = wid & 3, kh = wid >> 2;
  const int b = blockIdx.x, xcd = b & 7, slot = b >> 3, rg = xcd * 4 + (slot >> 3), hg = slot & 7;
  const int kvmt = hg >> 1, isV = hg & 1, rank = (mt - kvmt - 1) & 3;
  const bool pub = (wid == kvmt);
  const unsigned lds0 = (unsigned)(uintptr_t)lds;
  const unsigned tag = *(const __attribute__((address_space(4))) unsigned*)(ws + WS_EPOCH) + 1u;
  STAMP_DECL
  STAMP(0);

  const bool team = (rank == 3 || rank == 0);
  const int tw = (rank == 3 ? 0 : 2) + kh;
  const int bw = (rank == 1 ? 0 : 2) + kh;
  LAS unsigned* cnt = (LAS unsigned*)((LAS char*)lds + L_CNT);
  if (tid == 0) { cnt[0] = 0; cnt[1] = 0; }
  BAR_LGKM();
  auto dma_x = [&](int i) {
    const int P = i * 64 + lane, row = P / 80, jp = P - row * 80, js = (jp & ~15) | ((jp ^ row) & 15);
    glds16((const char*)p.x + (size_t)(rg * 64 + row) * 1280 + js * 16, (unsigned)__builtin_amdgcn_readfirstlane(lds0 + L_X + i * 1024));
  };
  auto dma_w = [&](const u16* w, int i, int split, int r0, int r1, int ldsbase) {
    const int ci = i * 64 + lane, ar = ci / 10, cc = ci - ar * 10, row = ar < split ? ar + r0 : ar - split + r1;
    glds16((const char*)w + (size_t)row * 1280 + hg * 160 + cc * 16, (unsigned)__builtin_amdgcn_readfirstlane(lds0 + ldsbase + i * 1024));
  };
  auto team_sync = [&](int k) {
    if (lane == 0) __hip_atomic_fetch_add(cnt + k, 1u, __ATOMIC_RELAXED, __HIP_MEMORY_SCOPE_WORKGROUP);
    while (__hip_atomic_load(cnt + k, __ATOMIC_RELAXED, __HIP_MEMORY_SCOPE_WORKGROUP) < 4u) {}
    asm volatile("" ::: "memory");
  };
  const int q4 = c >> 2, p4 = c & 3;
  const int ro1 = 8 * g + (g & 1) * 4 + q4, ro2 = 8 * g + (1 - (g & 1)) * 4 + q4;
  const bf16x8 ones = AS8(((u32x4){0x3f803f80u, 0x3f803f80u, 0x3f803f80u, 0x3f803f80u}));
  u32x4 xf[10];
  auto read_x = [&]() {
    const int xrow = mt * 16 + c;
    lds_cptr xb = (lds_cptr)lds + L_X + xrow * 1280;
#pragma unroll
    for (int s = 0; s < 10; ++s) {
      const int j = kh * 40 + 4 * s + g, jp = (j & ~15) | ((j ^ xrow) & 15);
      u32x2 a = *(const LAS u32x2*)(xb + jp * 16 + (g & 1) * 8), bb = *(const LAS u32x2*)(xb + jp * 16 + 8 - (g & 1) * 8);
      xf[s] = (u32x4){a[0], a[1], bb[0], bb[1]};
    }
  };
  f32x2 st1;
  auto x_stats = [&]() {
    f32x4 sm = {}, gr = {};
#pragma unroll
    for (int s = 0; s < 10; ++s) { sm = MFMAS(ones, AS8(xf[s]), sm); gr = MFMAS(AS8(xf[s]), AS8(xf[s]), gr); }
    const float dg = p4 == 0 ? gr[0] : p4 == 1 ? gr[1] : p4 == 2 ? gr[2] : gr[3];
    st1 = (f32x2){sm[0], dg};
  };
  f32x4 ax[3] = {};
  float kn2 = 0.f;
  if (team) {
#pragma unroll
    for (int j = 0; j < 5; ++j) dma_x(20 * kvmt + tw + 4 * j);
    const char* src = (const char*)(isV ? p.wv : p.wk);
#pragma unroll
    for (int j = 0; j < 7; ++j) {
      const int i = tw + 4 * j;
      if (i < 25) glds16(src + i * 1024 + lane * 16, (unsigned)__builtin_amdgcn_readfirstlane(lds0 + L_WKV + i * 1024));
    }
    if (pub) {
      const unsigned nt = ~tag;
      char* st = ws + WS_ST + (size_t)(rg * 8 + hg) * 1536;
      st_g64(st + lane * 8, 0u, nt); st_g64(st + 512 + lane * 8, 0u, nt); st_g64(st + 1024 + lane * 8, 0u, nt);
      char* rw = ws + WS_ROW + (size_t)rg * 5120 + hg * 640;
      st_g64(rw + lane * 8, 0u, nt);
      if (lane < 16) st_g64(rw + 512 + lane * 8, 0u, nt);
      if (lane < 4 && b < 216) st_g64(ws + WS_ACT + (size_t)b * 32 + lane * 8, 0u, nt);
      if (lane == 4) st_g64(ws + WS_F2 + rg * 128 + hg * 8, 0u, nt);
      if (hg == 0 && (lane == 5 || lane == 6)) st_g64(ws + WS_CAND + rg * 16 + (lane - 5) * 8, 0u, nt);
    }
    DRAIN();
    team_sync(0);
    STAMP(20);
    lds_cptr k1 = (lds_cptr)lds + L_WKV + (320 * kh + ro1) * 40 + p4 * 8, k2 = (lds_cptr)lds + L_WKV + (320 * kh + ro2) * 40 + p4 * 8;
    bf16x8 wf[10][2];
    if (rank == 3) read_x();
#pragma unroll
    for (int s = 0; s < 10; ++s)
#pragma unroll
      for (int u = 0; u < 2; ++u) wf[s][u] = cat8(trread(k1 + s * 1280 + u * 32), trread(k2 + s * 1280 + u * 32));
    if (rank == 0) {
#pragma unroll
      for (int s = 0; s < 10; ++s) { ax[0] = MFMAS(wf[s][0], ones, ax[0]); ax[1] = MFMAS(wf[s][1], ones, ax[1]); }
      if (c == 0) { *(f32x4*)(lds + L_KCS + kh * 128 + g * 16) = ax[0]; *(f32x4*)(lds + L_KCS + kh * 128 + 64 + g * 16) = ax[1]; }
    } else {
      x_stats();
      if (isV) {
#pragma unroll
        for (int s = 0; s < 10; ++s) { ax[0] = MFMAS(AS8(xf[s]), wf[s][0], ax[0]); ax[1] = MFMAS(AS8(xf[s]), wf[s][1], ax[1]); }
      } else {
#pragma unroll
        for (int s = 0; s < 10; ++s) { ax[0] = MFMAS(wf[s][0], AS8(xf[s]), ax[0]); ax[1] = MFMAS(wf[s][1], AS8(xf[s]), ax[1]); }
      }
      if (g == q4) *(f32x2*)(lds + L_KST + (kh * 16 + c) * 8) = st1;
      if (kh == 1) { *(f32x4*)(lds + L_KVX + lane * 16) = ax[0]; *(f32x4*)(lds + L_KVX + 1024 + lane * 16) = ax[1]; }
    }
    asm volatile("s_waitcnt lgkmcnt(0)" ::: "memory");
    team_sync(1);
    STAMP(21);
    if (pub) {
      auto kv_stats = [&](int pp, float& mean, float& rstd) {
        f32x2 a = *(const f32x2*)(lds + L_KST + pp * 8), bb = *(const f32x2*)(lds + L_KST + (16 + pp) * 8);
        mean = (a[0] + bb[0]) * (1.0f / D);
        rstd = __builtin_amdgcn_rsqf(fmaxf((a[1] + bb[1]) * (1.0f / D) - mean * mean, 0.f) + LN_EPS);
      };
      const float* kcs = (const float*)(lds + L_KCS);
      f32x4 a0 = ax[0] + *(const f32x4*)(lds + L_KVX + lane * 16), a1 = ax[1] + *(const f32x4*)(lds + L_KVX + 1024 + lane * 16);
      const int gm = rg * 4 + kvmt;
      char* tb = ws + WS_KV + (size_t)(gm >> 1) * TILE_B;
      if (!isV) {
        float mean, rstd; kv_stats(c, mean, rstd);
        f32x4 c0 = *(const f32x4*)(kcs + 4 * g) + *(const f32x4*)(kcs + 32 + 4 * g);
        f32x4 c1 = *(const f32x4*)(kcs + 16 + 4 * g) + *(const f32x4*)(kcs + 48 + 4 * g);
        unsigned w0 = cvtpk((a0[0] - mean * c0[0]) * rstd, (a0[1] - mean * c0[1]) * rstd), w1 = cvtpk((a0[2] - mean * c0[2]) * rstd, (a0[3] - mean * c0[3]) * rstd);
        unsigned w2 = cvtpk((a1[0] - mean * c1[0]) * rstd, (a1[1] - mean * c1[1]) * rstd), w3 = cvtpk((a1[2] - mean * c1[2]) * rstd, (a1[3] - mean * c1[3]) * rstd);
        const int rr = (gm & 1) * 16 + c;
        st_g64(tb + ((g >> 1) * 32 + rr) * 16 + (g & 1) * 8, w0, w1);
        float n2 = dot2(w0, w0, dot2(w1, w1, 0.f));
        if (g == 0) { st_g64(tb + 1024 + rr * 8, w2, w3); n2 = dot2(w2, w2, dot2(w3, w3, n2)); }
        n2 += __shfl_xor(n2, 16); n2 += __shfl_xor(n2, 32);
        n2 = fmaxf(n2, __shfl_xor(n2, 1)); n2 = fmaxf(n2, __shfl_xor(n2, 2)); n2 = fmaxf(n2, __shfl_xor(n2, 4)); n2 = fmaxf(n2, __shfl_xor(n2, 8));
        kn2 = n2;
      } else {
        const float cs0 = kcs[c] + kcs[32 + c], cs1 = kcs[16 + c] + kcs[48 + c];
        float v0[4], v1[4];
#pragma unroll
        for (int i = 0; i < 4; ++i) {
          float mean, rstd; kv_stats(4 * g + i, mean, rstd);
          v0[i] = (a0[i] - mean * cs0) * rstd; v1[i] = (a1[i] - mean * cs1) * rstd;
        }
        char* vb = tb + 1280 + (((gm & 1) * 2 + (g & 1)) * 21) * 16 + (g >> 1) * 8;
        st_g64(vb + c * 16, cvtpk(v0[0], v0[1]), cvtpk(v0[2], v0[3]));
        if (c < 5) st_g64(vb + (16 + c) * 16, c < 4 ? cvtpk(v1[0], v1[1]) : 0x3f803f80u, c < 4 ? cvtpk(v1[2], v1[3]) : 0x3f803f80u);
      }
    }
    ax[0] = (f32x4){}; ax[1] = (f32x4){};
  } else {
#pragma unroll
    for (int j = 0; j < 15; ++j) { int i = 20 * kvmt + 20 + bw + 4 * j; i = i >= 80 ? i - 80 : i; dma_x(i); }
#pragma unroll
    for (int j = 0; j < 13; ++j) {
      const int idx = bw + 4 * j;
      if (idx < 50) dma_w(p.wq, (idx & 1) * 25 + (idx >> 1), 160, 0, 320, L_WQA);
    }
    asm volatile("s_waitcnt vmcnt(12)" ::: "memory");
  }
  BAR_LGKM();
  STAMP(1);
  if (rank != 3) { read_x(); x_stats(); }
  if (g == q4) *(f32x2*)(lds + L_ST1 + (wid * 16 + c) * 8) = st1;
  BAR_LGKM();
  STAMP(2);
  if (pub) {
    DRAIN();
    if (lane < 2) st_g64(ws + WS_F1 + b * 16 + lane * 8, lane == 0 ? __float_as_uint(kn2) : ~tag ^ 0x5a5a5a5au, tag);
  } else {
    const int w7 = wid - (wid > kvmt ? 1 : 0);
#pragma unroll
    for (int j = 0; j < 8; ++j) {
      const int idx = w7 + 7 * j;
      if (idx < 50) dma_w(p.wq, (idx & 1) * 25 + (idx >> 1), 160, 160, 480, L_WQB);
    }
    asm volatile("s_waitcnt vmcnt(7)" ::: "memory");
  }
  asm volatile("s_barrier" ::: "memory");
  STAMP(3);
  float* csp = (float*)(lds + L_CS);
  auto row_stats = [&](int m, int pp, float& mean, float& rstd) {
    f32x2 a = *(const f32x2*)(lds + L_ST1 + (m * 16 + pp) * 8), bb = *(const f32x2*)(lds + L_ST1 + ((m + 4) * 16 + pp) * 8);
    mean = (a[0] + bb[0]) * (1.0f / D);
    rstd = __builtin_amdgcn_rsqf(fmaxf((a[1] + bb[1]) * (1.0f / D) - mean * mean, 0.f) + LN_EPS);
  };
  f32x4 acc[5] = {};
  bf16x8 kl[8], va[8], vb[8]; bf16x4 kx[8];
  const __amdgpu_buffer_rsrc_t rskv = __builtin_amdgcn_make_buffer_rsrc((void*)(ws + WS_KV), 0, 64 * TILE_B, 0x00020000);
  const unsigned kvb = wid * 8 * TILE_B;
  const unsigned oKA = kvb + lane * 16, oKB = kvb + (h == 0 ? 1024 + r * 8 : 1280 + 20 * 16);
  const unsigned oV = r <= 20 ? kvb + 1280 + (h * 21 + r) * 16 : 0x40000000u;
#define LOAD_KV(i) do { \
      kl[i] = AS8(__builtin_amdgcn_raw_buffer_load_b128(rskv, oKA + (i) * TILE_B, 0, 16)); \
      kx[i] = AS4(__builtin_amdgcn_raw_buffer_load_b64(rskv, oKB + (i) * TILE_B, 0, 16)); \
      va[i] = AS8(__builtin_amdgcn_raw_buffer_load_b128(rskv, oV + (i) * TILE_B, 0, 16)); \
      vb[i] = AS8(__builtin_amdgcn_raw_buffer_load_b128(rskv, oV + (i) * TILE_B + 672, 0, 16)); } while (0)
#define QSTEPS(base, s0, KV) do { \
    lds_cptr w1 = (lds_cptr)lds + (base) + (160 * kh + ro1) * 160 + p4 * 8, w2 = (lds_cptr)lds + (base) + (160 * kh + ro2) * 160 + p4 * 8; \
    bf16x8 wf[2][5]; \
    _Pragma("unroll") for (int t = 0; t < 5; ++t) wf[0][t] = cat8(trread(w1 + t * 32), trread(w2 + t * 32)); \
    _Pragma("unroll") for (int s = 0; s < 5; ++s) { \
      if (s + 1 < 5) { _Pragma("unroll") for (int t = 0; t < 5; ++t) wf[(s + 1) & 1][t] = cat8(trread(w1 + (s + 1) * 5120 + t * 32), trread(w2 + (s + 1) * 5120 + t * 32)); } \
      SBAR(); \
      const bf16x8 xs = AS8(xf[(s0) + s]); \
      _Pragma("unroll") for (int t = 0; t < 5; ++t) acc[t] = MFMAS(wf[s & 1][t], xs, acc[t]); \
      if (rank == 0) ax[2] = MFMAS(wf[s & 1][0], ones, ax[2]); \
      else if (rank == 1) { ax[0] = MFMAS(wf[s & 1][1], ones, ax[0]); ax[1] = MFMAS(wf[s & 1][2], ones, ax[1]); } \
      else if (rank == 2) { ax[0] = MFMAS(wf[s & 1][3], ones, ax[0]); ax[1] = MFMAS(wf[s & 1][4], ones, ax[1]); } \
      SBAR(); \
      if (KV) { if (s < 4) { LOAD_KV(2 * (s & 3)); LOAD_KV(2 * (s & 3) + 1); } SBAR(); } \
    } } while (0)
  QSTEPS(L_WQA, 0, false);
  STAMP(17);
  if (pub) {
    float km = 0.f;
    for (unsigned spins = 0; spins < SPIN_CAP; ++spins) {
      bool ok = true; km = 0.f;
#pragma unroll
      for (int i = 0; i < 4; ++i) {
        const u64 a = ld_g64(ws + WS_F1 + (i * 64 + lane) * 16), bb = ld_g64(ws + WS_F1 + (i * 64 + lane) * 16 + 8);
        ok &= ((unsigned)(a >> 32) == tag) & ((unsigned)(bb >> 32) == tag) & ((unsigned)bb == (~tag ^ 0x5a5a5a5au));
        km = fmaxf(km, __uint_as_float((unsigned)a));
      }
      if (__all(ok)) break;
      __builtin_amdgcn_s_sleep(1);
    }
#pragma unroll
    for (int o = 1; o < 64; o <<= 1) km = fmaxf(km, __shfl_xor(km, o));
    if (lane == 0) *(float*)(lds + L_MISC) = km;
    if (b == 0 && lane == 0) __hip_atomic_store((gu32*)(ws + WS_EPOCH), tag, RLX_AGENT);
  } else {
    DRAIN();
  }
  BAR_LGKM();
  STAMP(4);
  QSTEPS(L_WQB, 5, true);
  STAMP(15);
  if (c == 0) {
    if (rank == 0) *(f32x4*)(csp + (kh * 5 + 0) * 16 + 4 * g) = ax[2];
    else if (rank == 1) { *(f32x4*)(csp + (kh * 5 + 1) * 16 + 4 * g) = ax[0]; *(f32x4*)(csp + (kh * 5 + 2) * 16 + 4 * g) = ax[1]; }
    else if (rank == 2) { *(f32x4*)(csp + (kh * 5 + 3) * 16 + 4 * g) = ax[0]; *(f32x4*)(csp + (kh * 5 + 4) * 16 + 4 * g) = ax[1]; }
  }
  {
    char* qx = lds + L_QX + mt * 5120;
    if (kh == 0) { *(f32x4*)(qx + 3 * 1024 + lane * 16) = acc[3]; *(f32x4*)(qx + 4 * 1024 + lane * 16) = acc[4]; }
    else { *(f32x4*)(qx + 0 * 1024 + lane * 16) = acc[0]; *(f32x4*)(qx + 1 * 1024 + lane * 16) = acc[1]; *(f32x4*)(qx + 2 * 1024 + lane * 16) = acc[2]; }
    const float km2 = *(const float*)(lds + L_MISC);
    float mean, rstd; row_stats(mt, c, mean, rstd);
    rstd *= QSCALE;
    BAR_LGKM();
    const int ph = mt >> 1, rr = (mt & 1) * 16 + c;
    auto fin = [&](int t, f32x4 a) {
      a += *(const f32x4*)(qx + t * 1024 + lane * 16);
      const f32x4 csv = *(const f32x4*)(csp + t * 16 + 4 * g) + *(const f32x4*)(csp + (5 + t) * 16 + 4 * g);
      const int n = 16 * t + 4 * g, hh = n / 20, d = n - hh * 20;
      char* qt = lds + L_Q + (hh * 2 + ph) * 1280;
      char* dst = d < 16 ? qt + ((d >> 3) * 32 + rr) * 16 + (d & 4) * 2 : qt + 1024 + rr * 8;
      *(u32x2*)dst = (u32x2){cvtpk((a[0] - mean * csv[0]) * rstd, (a[1] - mean * csv[1]) * rstd), cvtpk((a[2] - mean * csv[2]) * rstd, (a[3] - mean * csv[3]) * rstd)};
    };
    if (kh == 0) { fin(0, acc[0]); fin(1, acc[1]); fin(2, acc[2]); }
    else { fin(3, acc[3]); fin(4, acc[4]); }
    BAR_LGKM();
    const u32x4 ql = *(const u32x4*)(lds + L_Q + wid * 1280 + lane * 16);
    const u32x2 qh = *(const u32x2*)(lds + L_Q + wid * 1280 + 1024 + r * 8);
    float n2 = dot2(ql[0], ql[0], dot2(ql[1], ql[1], 0.f)) + dot2(ql[2], ql[2], dot2(ql[3], ql[3], 0.f));
    if (h == 0) n2 += dot2(qh[0], qh[0], dot2(qh[1], qh[1], 0.f));
    n2 += __shfl_xor(n2, 32);
    const float M = __builtin_amdgcn_sqrtf(n2 * km2) * 1.0078125f;
    if (h == 0) *(u32x2*)(lds + L_QB + wid * 256 + r * 8) = (u32x2){cvtpk(-M, 0.f), 0u};
  }
  STAMP(16);
  unsigned pf_voff, pf_lds, pf_n; const char* pf_src; unsigned pf_step;
  if (wid < 5) {
    const int ci = wid * 64 + lane, rw = ci / 10, cc = ci - rw * 10;
    pf_voff = rw * 1280 + hg * 160 + cc * 16; pf_src = (const char*)p.wo; pf_step = 32 * 1280; pf_lds = lds0 + L_WOA + wid * 1024; pf_n = 20;
  } else if (wid == 5) {
    pf_voff = lane * (FF * 2) + b * 16; pf_src = (const char*)p.wg; pf_step = 64 * FF * 2; pf_lds = lds0 + L_FGU; pf_n = b < 216 ? 20 : 0;
  } else {
    const int p0_ = wid == 6 ? 0 : 14;
    pf_voff = lane * 1280 + (b - 176) * 16; pf_src = (const char*)p.wd + (size_t)p0_ * 64 * 1280; pf_step = 64 * 1280; pf_lds = lds0 + L_FD + p0_ * 1024;
    pf_n = b >= 176 ? (wid == 6 ? 14 : 13) : 0;
  }
  unsigned pf_i = 0;
  auto pf_issue = [&]() {
    if (pf_i < pf_n) {
      const char* src = pf_src + (size_t)pf_i * pf_step;
      unsigned dst = pf_lds + pf_i * (wid < 5 ? 5120u : 1024u);
      if (wid < 5) {
        if (pf_i >= 5) src += 160 * 1280;
        if (pf_i >= 10) { src = pf_src + (size_t)(pf_i < 15 ? 160 + (pf_i - 10) * 32 : 480 + (pf_i - 15) * 32) * 1280; dst = lds0 + L_DUM + (wid & 1) * 1024; }
      } else if (wid == 5 && pf_i >= 10) src = (const char*)p.wu + (size_t)(pf_i - 10) * pf_step;
      unsigned sv;
      asm volatile("s_mov_b32 %0, m0\n\ts_mov_b32 m0, %3\n\ts_nop 0\n\tglobal_load_lds_dwordx4 %1, %2\n\ts_mov_b32 m0, %0" : "=&s"(sv) : "v"(pf_voff), "s"(src), "s"(dst) : "memory");
      ++pf_i;
    }
    SBAR();
  };
#pragma unroll
  for (int i = 0; i < 8; ++i) { PIN(kl[i]); PIN(kx[i]); PIN(va[i]); PIN(vb[i]); }
  STAMP(5);
  const unsigned oQL = L_Q + lane * 16, oQH = h == 0 ? L_Q + 1024 + r * 8 : L_QB + r * 8;
  const unsigned sQH = h == 0 ? 1280 : 256;
  f32x16 F;
  volatile LAS int* redo = (volatile LAS int*)((lds_cptr)lds + L_REDO);
#pragma unroll 1
  for (int pass = 0; pass < 2; ++pass) {
    BAR_LGKM();
    f32x16 OA = {}, OB = {}, SA, SB;
    u32x4 p0, p1 = {0, 0, 0, 0};
    bf16x8 qcl, qnl; bf16x4 qch, qnh;
    qcl = *(const LAS bf16x8*)((lds_cptr)lds + oQL + wid * 1280);
    qch = *(const LAS bf16x4*)((lds_cptr)lds + oQH + wid * sQH);
    SA = MFMA8(kx[0], qch, MFMA16(kl[0], qcl, (f32x16){}));
    SBAR();
#define EXG(X, i, P, j) do { X[i] = __builtin_amdgcn_exp2f(X[i]); X[i + 1] = __builtin_amdgcn_exp2f(X[i + 1]); P[j] = cvtpk(X[i], X[i + 1]); \
                             X[i + 2] = __builtin_amdgcn_exp2f(X[i + 2]); X[i + 3] = __builtin_amdgcn_exp2f(X[i + 3]); P[j + 1] = cvtpk(X[i + 2], X[i + 3]); PIN(P); SBAR(); } while (0)
#define STEP(C, N, i, OP, OC, OCIN, QL, QH, EXTRA) do { \
      N = MFMA16(kl[((i) + 1) & 7], QL, (f32x16){}); PIN(N); SBAR(); \
      EXG(C, 0, p0, 0); \
      OP = MFMA16(vb[((i) + 7) & 7], AS8(p1), OP); PIN(OP); SBAR(); \
      EXG(C, 4, p0, 2); \
      N = MFMA8(kx[((i) + 1) & 7], QH, N); PIN(N); SBAR(); \
      EXTRA; \
      EXG(C, 8, p1, 0); \
      OC = MFMA16(va[i], AS8(p0), OCIN); PIN(OC); SBAR(); \
      EXG(C, 12, p1, 2); \
    } while (0)
#define WR_O(X, par) do { LAS char* d_ = (LAS char*)lds + L_OA + ((((wid + t + 7) & 7) * 2 + (par)) * 3072) + lane * 16; \
      *(LAS f32x4*)(d_) = (f32x4){X[0], X[1], X[2], X[3]}; *(LAS f32x4*)(d_ + 1024) = (f32x4){X[4], X[5], X[6], X[7]}; \
      *(LAS f32x4*)(d_ + 2048) = (f32x4){X[8], X[9], X[10], X[11]}; PIN(X); SBAR(); } while (0)
#define RD_O(X, par) do { const int jn_ = (t == 7) ? ((wid + 7) & 7) : ((wid + t + 1) & 7); \
      lds_cptr s_ = (lds_cptr)lds + L_OA + ((jn_ * 2 + (par)) * 3072) + lane * 16; \
      const f32x4 a_ = *(const LAS f32x4*)(s_), b_ = *(const LAS f32x4*)(s_ + 1024), c_ = *(const LAS f32x4*)(s_ + 2048); \
      X[0] = a_[0]; X[1] = a_[1]; X[2] = a_[2]; X[3] = a_[3]; X[4] = b_[0]; X[5] = b_[1]; X[6] = b_[2]; X[7] = b_[3]; \
      X[8] = c_[0]; X[9] = c_[1]; X[10] = c_[2]; X[11] = c_[3]; \
      const int jq_ = (wid + t + 1) & 7; \
      qnl = *(const LAS bf16x8*)((lds_cptr)lds + oQL + jq_ * 1280); qnh = *(const LAS bf16x4*)((lds_cptr)lds + oQH + jq_ * sQH); SBAR(); } while (0)
#define ROUND(OC, OP, par) do { \
      STEP(SA, SB, 0, OP, OC, OC, qcl, qch, ); \
      STEP(SB, SA, 1, OC, OC, OC, qcl, qch, WR_O(OP, par)); \
      STEP(SA, SB, 2, OC, OC, OC, qcl, qch, BAR_LGKM(); SBAR()); \
      STEP(SB, SA, 3, OC, OC, OC, qcl, qch, RD_O(OP, par)); \
      STEP(SA, SB, 4, OC, OC, OC, qcl, qch, pf_issue()); \
      STEP(SB, SA, 5, OC, OC, OC, qcl, qch, pf_issue()); \
      STEP(SA, SB, 6, OC, OC, OC, qcl, qch, pf_issue()); \
      STEP(SB, SA, 7, OC, OC, OC, qnl, qnh, ); \
      qcl = qnl; qch = qnh; \
    } while (0)
#pragma unroll 1
    for (int t = 0; t < 8; ++t) {
      ROUND(OA, OB, 1);
      ++t;
      ROUND(OB, OA, 0);
    }
    OB = MFMA16(vb[7], AS8(p1), OB);
#pragma unroll
    for (int i = 0; i < 12; ++i) F[i] = OB[i] + OA[i];
    if (pass == 1) break;
    const bool bad = (h == 1) && !(F[8] >= 5.421010862427522e-20f);
    if (__builtin_expect(!__any(bad), 1)) {   }
    if (tid == 0) *redo = 0;
    BAR_LGKM();
    if (__any(bad) && lane == 0) *redo = 1;
    BAR_LGKM();
    if (*redo == 0) break;
    {
      float* mx = (float*)(lds + L_OA);
#pragma unroll 1
      for (int j = 0; j < 8; ++j) {
        const bf16x8 ql = *(const LAS bf16x8*)((lds_cptr)lds + oQL + j * 1280);
        bf16x4 qh = *(const LAS bf16x4*)((lds_cptr)lds + L_Q + 1024 + r * 8 + j * 1280);
        if (h == 1) qh = (bf16x4){};
        float m = -INFINITY;
#pragma unroll
        for (int i = 0; i < 8; ++i) {
          const f32x16 sc = MFMA8(kx[i], qh, MFMA16(kl[i], ql, (f32x16){}));
#pragma unroll
          for (int e = 0; e < 16; ++e) m = fmaxf(m, sc[e]);
        }
        m = fmaxf(m, __shfl_xor(m, 32));
        if (h == 0) mx[(wid * 8 + j) * 32 + r] = m;
      }
      BAR_LGKM();
      float m = -INFINITY;
#pragma unroll
      for (int w = 0; w < 8; ++w) m = fmaxf(m, mx[(w * 8 + wid) * 32 + r]);
      const float nm = -m, a = bfr(nm), r1 = nm - a, bb = bfr(r1), cc = r1 - bb;
      if (h == 0) *(u32x2*)(lds + L_QB + wid * 256 + r * 8) = (u32x2){cvtpk(a, bb), cvtpk(cc, 0.f)};
    }
  }
  STAMP(6);
  {
    const int j = (wid + 7) & 7, hh = j >> 1, pos = (j & 1) * 32 + r;
    const float l = __shfl(F[8], r + 32);
    const float rl = 1.0f / l;
    char* ob = ws + WS_O + (size_t)rg * 81920 + (size_t)(pos >> 4) * 20480 + (pos & 15) * 16;
    auto put = [&](int d, float a0, float a1, float a2, float a3) {
      const int col = hg * 80 + hh * 20 + d, oc = col >> 3, gg = oc & 3, half = ((col >> 2) & 1) ^ (gg & 1);
      st_g64(ob + (oc >> 2) * 1024 + gg * 256 + half * 8, cvtpk(a0 * rl, a1 * rl), cvtpk(a2 * rl, a3 * rl));
    };
    put(4 * h, F[0], F[1], F[2], F[3]);
    put(8 + 4 * h, F[4], F[5], F[6], F[7]);
    if (h == 0) put(16, F[8], F[9], F[10], F[11]);
  }
  DRAIN();
  asm volatile("s_barrier" ::: "memory");
  if (tid == 0) st_g64(ws + WS_F2 + rg * 128 + hg * 8, ~tag, tag);
#pragma unroll
  for (int j = 0; j < 7; ++j) { const int i = wid + 8 * j; if (i < 50) dma_w(p.wo, i, 160, 160, 480, L_WOB); }
  STAMP(7);
  const int row = rg * 64 + mt * 16 + c;
  const int t0 = kh == 0 ? 0 : 3;
  u32x2 xv[3], wv[3];
#pragma unroll
  for (int tt = 0; tt < 3; ++tt) {
    const int t = (t0 + tt) > 4 ? 4 : t0 + tt, col = hg * 80 + t * 16 + 4 * g;
    xv[tt] = *(const u32x2*)(p.x + (size_t)row * D + col);
    wv[tt] = *(const u32x2*)(p.wr + col);
  }
  float sw;
  {
    const unsigned* wp = (const unsigned*)p.wr;
    sw = 0.f;
#pragma unroll
    for (int i = 0; i < 5; ++i) sw = dot2(wp[lane + 64 * i], 0x3f803f80u, sw);
  }
  for (unsigned spins = 0; spins < SPIN_CAP; ++spins) {
    const u64 f = ld_g64(ws + WS_F2 + rg * 128 + (kh * 4 + (lane & 3)) * 8);
    if (__all(f == (((u64)tag << 32) | (unsigned)~tag))) break;
    __builtin_amdgcn_s_sleep(1);
  }
  SBAR();
  STAMP(18);
  u32x4 of[10];
  {
    const __amdgpu_buffer_rsrc_t rs = __builtin_amdgcn_make_buffer_rsrc((void*)(ws + WS_O + (size_t)rg * 81920), 0, 81920, 0x00020000);
#pragma unroll
    for (int s = 0; s < 10; ++s) of[s] = __builtin_amdgcn_raw_buffer_load_b128(rs, (mt * 20 + kh * 10 + s) * 1024 + lane * 16, 0, 16);
  }
  SBAR();
  STAMP(8);
#pragma unroll
  for (int t = 0; t < 5; ++t) acc[t] = (f32x4){};
#define OSTEPS(base, rows, s0, ns) do { \
    lds_cptr w1 = (lds_cptr)lds + (base) + ((rows) * kh + ro1) * 160 + p4 * 8, w2 = (lds_cptr)lds + (base) + ((rows) * kh + ro2) * 160 + p4 * 8; \
    bf16x8 wf[2][5]; \
    _Pragma("unroll") for (int t = 0; t < 5; ++t) wf[0][t] = cat8(trread(w1 + t * 32), trread(w2 + t * 32)); \
    _Pragma("unroll") for (int s = 0; s < (ns); ++s) { \
      if (s + 1 < (ns)) { _Pragma("unroll") for (int t = 0; t < 5; ++t) wf[(s + 1) & 1][t] = cat8(trread(w1 + (s + 1) * 5120 + t * 32), trread(w2 + (s + 1) * 5120 + t * 32)); } \
      SBAR(); \
      _Pragma("unroll") for (int t = 0; t < 5; ++t) acc[t] = MFMAS(wf[s & 1][t], AS8(of[(s0) + s]), acc[t]); \
      SBAR(); \
    } } while (0)
  OSTEPS(L_WOA, 160, 0, 5);
  BAR_LGKM();
  OSTEPS(L_WOB, 160, 5, 5);
  STAMP(19);
  unsigned hw[3][2];
  {
    char* qx = lds + L_EX + mt * 5120;
    if (kh == 0) { *(f32x4*)(qx + 3 * 1024 + lane * 16) = acc[3]; *(f32x4*)(qx + 4 * 1024 + lane * 16) = acc[4]; }
    else { *(f32x4*)(qx + 0 * 1024 + lane * 16) = acc[0]; *(f32x4*)(qx + 1 * 1024 + lane * 16) = acc[1]; *(f32x4*)(qx + 2 * 1024 + lane * 16) = acc[2]; }
    BAR_LGKM();
    const int nt = kh == 0 ? 3 : 2;
    float s1 = 0.f, dt = 0.f;
#pragma unroll
    for (int tt = 0; tt < 3; ++tt) {
      const int t = (t0 + tt) > 4 ? 4 : t0 + tt;
      const f32x4 a = (kh == 0 ? acc[tt] : acc[tt + 3 > 4 ? 4 : tt + 3]) + *(const f32x4*)(qx + t * 1024 + lane * 16);
      const unsigned a01 = cvtpk(a[0], a[1]), a23 = cvtpk(a[2], a[3]);
      hw[tt][0] = cvtpk(bflo(xv[tt][0]) + bflo(a01), bfhi(xv[tt][0]) + bfhi(a01));
      hw[tt][1] = cvtpk(bflo(xv[tt][1]) + bflo(a23), bfhi(xv[tt][1]) + bfhi(a23));
      if (tt < nt) {
        s1 = dot2(hw[tt][0], 0x3f803f80u, dot2(hw[tt][1], 0x3f803f80u, s1));
        dt = dot2(hw[tt][0], wv[tt][0], dot2(hw[tt][1], wv[tt][1], dt));
      }
    }
    s1 += __shfl_xor(s1, 16); dt += __shfl_xor(dt, 16);
    s1 += __shfl_xor(s1, 32); dt += __shfl_xor(dt, 32);
    const float ml = s1 * (kh == 0 ? 1.0f / 48.f : 1.0f / 32.f);
    float m2 = 0.f;
#pragma unroll
    for (int tt = 0; tt < 3; ++tt) {
      if (tt < nt) {
        const float d0 = bflo(hw[tt][0]) - ml, d1 = bfhi(hw[tt][0]) - ml, d2 = bflo(hw[tt][1]) - ml, d3 = bfhi(hw[tt][1]) - ml;
        m2 += (d0 * d0 + d1 * d1) + (d2 * d2 + d3 * d3);
      }
    }
    m2 += __shfl_xor(m2, 16); m2 += __shfl_xor(m2, 32);
    f32x4* st = (f32x4*)(lds + L_ST2);
    if (g == 0) st[wid * 16 + c] = (f32x4){s1, m2, dt, ml};
    BAR_LGKM();
    if (wid == 0) {
      const f32x4 a = st[lane], bb = st[64 + lane];
      const float mean = (a[0] + bb[0]) * (1.0f / 80.f);
      const float da = a[3] - mean, db = bb[3] - mean;
      const float M2 = a[1] + bb[1] + 48.f * da * da + 32.f * db * db;
      char* sp = ws + WS_ST + (size_t)(rg * 8 + hg) * 1536 + lane * 8;
      st_g64(sp, __float_as_uint(mean), tag); st_g64(sp + 512, __float_as_uint(M2), tag); st_g64(sp + 1024, __float_as_uint(a[2] + bb[2]), tag);
    }
  }
  STAMP(9);
  {
    const char* sp = ws + WS_ST + (size_t)(rg * 8 + wid) * 1536 + lane * 8;
    u64 a, bb, cc;
    for (unsigned spins = 0; spins < SPIN_CAP; ++spins) {
      a = ld_g64(sp); bb = ld_g64(sp + 512); cc = ld_g64(sp + 1024);
      if (__all(((unsigned)(a >> 32) == tag) & ((unsigned)(bb >> 32) == tag) & ((unsigned)(cc >> 32) == tag))) break;
      __builtin_amdgcn_s_sleep(1);
    }
    float* gs = (float*)(lds + L_GS) + wid * 192 + lane;
    gs[0] = __uint_as_float((unsigned)a); gs[64] = __uint_as_float((unsigned)bb); gs[128] = __uint_as_float((unsigned)cc);
  }
#pragma unroll
  for (int o = 1; o < 64; o <<= 1) sw += __shfl_xor(sw, o);
  BAR_LGKM();
  STAMP(10);
  float mean_l, rstd_l, r_l; int best;
  {
    const float* gs = (const float*)(lds + L_GS) + lane;
    float mu[8], mean = 0.f, M2 = 0.f, dt = 0.f;
#pragma unroll
    for (int i = 0; i < 8; ++i) { mu[i] = gs[i * 192]; mean += mu[i]; M2 += gs[i * 192 + 64]; dt += gs[i * 192 + 128]; }
    mean *= 0.125f;
#pragma unroll
    for (int i = 0; i < 8; ++i) { const float d = mu[i] - mean; M2 += 80.f * d * d; }
    mean_l = mean; rstd_l = __builtin_amdgcn_rsqf(M2 * (1.0f / D) + LN_EPS);
    r_l = rstd_l * (dt - mean * sw);
    unsigned ub = __float_as_uint(r_l); ub = (ub & 0x80000000u) ? ~ub : (ub | 0x80000000u);
    u64 key = ((u64)ub << 6) | (unsigned)(63 - lane);
#pragma unroll
    for (int o = 1; o < 64; o <<= 1) { const u64 k2 = __shfl_xor(key, o); key = k2 > key ? k2 : key; }
    best = 63 - (int)(key & 63);
  }
  const float r_best = __shfl(r_l, best);
  if (hg == 0 && wid == 0 && lane < 2) st_g64(ws + WS_CAND + rg * 16 + lane * 8, lane == 0 ? __float_as_uint(r_best) : (unsigned)(rg * 64 + best), tag);
  const int lrow = mt * 16 + c;
  const float mean = __shfl(mean_l, lrow), rstd = __shfl(rstd_l, lrow);
  unsigned ow[3][2];
  {
    const int nt = kh == 0 ? 3 : 2;
#pragma unroll
    for (int tt = 0; tt < 3; ++tt) {
      if (tt < nt) {
        const int colb = (t0 + tt) * 16 + 4 * g;
        const float h0 = bflo(hw[tt][0]), h1 = bfhi(hw[tt][0]), h2 = bflo(hw[tt][1]), h3 = bfhi(hw[tt][1]);
        const unsigned n01 = cvtpk((h0 - mean) * rstd, (h1 - mean) * rstd), n23 = cvtpk((h2 - mean) * rstd, (h3 - mean) * rstd);
        ow[tt][0] = cvtpk(h0 + bflo(n01), h1 + bfhi(n01)); ow[tt][1] = cvtpk(h2 + bflo(n23), h3 + bfhi(n23));
        if (lrow == best) {
          char* rw = ws + WS_ROW + (size_t)rg * 5120 + (hg * 80 + colb) * 8;
          st_g64(rw, (hw[tt][0] & 0xffffu) | (n01 << 16), tag);
          st_g64(rw + 8, (hw[tt][0] >> 16) | (n01 & 0xffff0000u), tag);
          st_g64(rw + 16, (hw[tt][1] & 0xffffu) | (n23 << 16), tag);
          st_g64(rw + 24, (hw[tt][1] >> 16) | (n23 & 0xffff0000u), tag);
        } else {
          *(u32x2*)(p.out + (size_t)row * D + hg * 80 + colb) = (u32x2){ow[tt][0], ow[tt][1]};
        }
      }
    }
  }
  STAMP(11);
  if (wid == 0) {
    u64 a, bb;
    for (unsigned spins = 0; spins < SPIN_CAP; ++spins) {
      a = ld_g64(ws + WS_CAND + r * 16); bb = ld_g64(ws + WS_CAND + r * 16 + 8);
      if (__all(((unsigned)(a >> 32) == tag) & ((unsigned)(bb >> 32) == tag))) break;
      __builtin_amdgcn_s_sleep(1);
    }
    unsigned ub = (unsigned)a; ub = (ub & 0x80000000u) ? ~ub : (ub | 0x80000000u);
    u64 key = ((u64)ub << 5) | (unsigned)(31 - r);
#pragma unroll
    for (int o = 1; o < 32; o <<= 1) { const u64 k2 = __shfl_xor(key, o); key = k2 > key ? k2 : key; }
    if (lane == 31 - (int)(key & 31)) *(u32x2*)(lds + L_RED + 1024) = (u32x2){(unsigned)a, (unsigned)bb};
  }
  BAR_LGKM();
  const u32x2 win = *(const u32x2*)(lds + L_RED + 1024);
  const float r_sel = __uint_as_float(win[0]);
  const int widx = (int)win[1] & (S - 1), wrg = widx >> 6;
  if (wrg != rg && lrow == best) {
    const int nt = kh == 0 ? 3 : 2;
#pragma unroll
    for (int tt = 0; tt < 3; ++tt)
      if (tt < nt) *(u32x2*)(p.out + (size_t)row * D + hg * 80 + (t0 + tt) * 16 + 4 * g) = (u32x2){ow[tt][0], ow[tt][1]};
  }
  STAMP(12);
  if (b < 216) {
    const char* rw = ws + WS_ROW + (size_t)wrg * 5120;
    const int k0 = tid, k1 = tid < 128 ? tid + 512 : tid - 128, k2 = tid + 384 < 640 ? tid + 384 : 639;
    u64 a0, a1, a2;
    for (unsigned spins = 0; spins < SPIN_CAP; ++spins) {
      a0 = ld_g64(rw + k0 * 8); a1 = ld_g64(rw + k1 * 8); a2 = ld_g64(rw + k2 * 8);
      if (__all(((unsigned)(a0 >> 32) == tag) & ((unsigned)(a1 >> 32) == tag) & ((unsigned)(a2 >> 32) == tag))) break;
      __builtin_amdgcn_s_sleep(1);
    }
    u32x4 fw[3];
#pragma unroll
    for (int j = 0; j < 3; ++j) { int e = tid + 512 * j; e = e < 1280 ? e : 1279; fw[j] = *(const u32x4*)(lds + L_FGU + e * 16); }
    const float x0 = bfhi((unsigned)a0), x1 = bfhi((unsigned)a1), x2 = tid < 256 ? bfhi((unsigned)a2) : 0.f;
    float ga[8], ua[8];
    const float xg1 = tid < 128 ? x1 : 0.f, xu1 = tid < 128 ? 0.f : x1;
#pragma unroll
    for (int j = 0; j < 4; ++j) {
      ga[2 * j] = x0 * bflo(fw[0][j]) + xg1 * bflo(fw[1][j]); ga[2 * j + 1] = x0 * bfhi(fw[0][j]) + xg1 * bfhi(fw[1][j]);
      ua[2 * j] = xu1 * bflo(fw[1][j]) + x2 * bflo(fw[2][j]); ua[2 * j + 1] = xu1 * bfhi(fw[1][j]) + x2 * bfhi(fw[2][j]);
    }
    float v8[8], v4[4], v2[2], v1;
    { const bool up = lane & 1;
#pragma unroll
      for (int j = 0; j < 8; ++j) { const float keep = up ? ua[j] : ga[j], send = up ? ga[j] : ua[j]; v8[j] = keep + __shfl_xor(send, 1); } }
    { const bool up = lane & 2;
#pragma unroll
      for (int j = 0; j < 4; ++j) { const float keep = up ? v8[4 + j] : v8[j], send = up ? v8[j] : v8[4 + j]; v4[j] = keep + __shfl_xor(send, 2); } }
    { const bool up = lane & 4;
#pragma unroll
      for (int j = 0; j < 2; ++j) { const float keep = up ? v4[2 + j] : v4[j], send = up ? v4[j] : v4[2 + j]; v2[j] = keep + __shfl_xor(send, 4); } }
    { const bool up = lane & 8; const float keep = up ? v2[1] : v2[0], send = up ? v2[0] : v2[1]; v1 = keep + __shfl_xor(send, 8); }
    v1 += __shfl_xor(v1, 16); v1 += __shfl_xor(v1, 32);
    float* red = (float*)(lds + L_RED);
    if (lane < 16) red[wid * 16 + lane] = v1;
    BAR_LGKM();
    if (wid == 0) {
      float tot = 0.f;
#pragma unroll
      for (int w = 0; w < 8; ++w) tot += red[w * 16 + (lane & 15)];
      tot = bfr(tot);
      const float other = __shfl_xor(tot, 1);
      const float gv = (lane & 1) ? other : tot, uv = (lane & 1) ? tot : other;
      const float sg = bfr(gv / (1.0f + __expf(-gv)));
      const float act = bfr(sg * uv);
      const float nb = __shfl_xor(act, 8);
      if (lane < 8 && !(lane & 1)) {
        const int colp = ((lane >> 1) & 1) * 2 + ((lane >> 2) & 1);
        st_g64(ws + WS_ACT + (size_t)b * 32 + colp * 8, cvtpk(act, nb), tag);
      }
    }
  }
  if (b >= 176) {
    const int tq = tid < 432 ? tid : 431;
    const int colb = (b - 176) * 8;
    u32x4 fw[4];
#pragma unroll
    for (int j = 0; j < 4; ++j) fw[j] = *(const u32x4*)(lds + L_FD + (4 * tq + j) * 16);
    u64 hrow = 0;
    u64 a0, a1;
    for (unsigned spins = 0; spins < SPIN_CAP; ++spins) {
      a0 = ld_g64(ws + WS_ACT + tq * 16); a1 = ld_g64(ws + WS_ACT + tq * 16 + 8);
      hrow = ld_g64(ws + WS_ROW + (size_t)wrg * 5120 + (colb + (lane & 7)) * 8);
      if (__all(((unsigned)(a0 >> 32) == tag) & ((unsigned)(a1 >> 32) == tag) & ((unsigned)(hrow >> 32) == tag))) break;
      __builtin_amdgcn_s_sleep(1);
    }
    float av[4] = {bflo((unsigned)a0), bfhi((unsigned)a0), bflo((unsigned)a1), bfhi((unsigned)a1)};
    if (tid >= 432) { av[0] = av[1] = av[2] = av[3] = 0.f; }
    float y[8];
#pragma unroll
    for (int j = 0; j < 4; ++j) {
      y[2 * j] = (av[0] * bflo(fw[0][j]) + av[1] * bflo(fw[1][j])) + (av[2] * bflo(fw[2][j]) + av[3] * bflo(fw[3][j]));
      y[2 * j + 1] = (av[0] * bfhi(fw[0][j]) + av[1] * bfhi(fw[1][j])) + (av[2] * bfhi(fw[2][j]) + av[3] * bfhi(fw[3][j]));
    }
    float v4[4], v2[2], v1;
    { const bool up = lane & 1;
#pragma unroll
      for (int j = 0; j < 4; ++j) { const float keep = up ? y[4 + j] : y[j], send = up ? y[j] : y[4 + j]; v4[j] = keep + __shfl_xor(send, 1); } }
    { const bool up = lane & 2;
#pragma unroll
      for (int j = 0; j < 2; ++j) { const float keep = up ? v4[2 + j] : v4[j], send = up ? v4[j] : v4[2 + j]; v2[j] = keep + __shfl_xor(send, 2); } }
    { const bool up = lane & 4; const float keep = up ? v2[1] : v2[0], send = up ? v2[0] : v2[1]; v1 = keep + __shfl_xor(send, 4); }
    v1 += __shfl_xor(v1, 8); v1 += __shfl_xor(v1, 16); v1 += __shfl_xor(v1, 32);
    const int mycol = ((lane & 1) << 2) | (lane & 2) | ((lane & 4) >> 2);
    float* red = (float*)(lds + L_RED + 512);
    if (lane < 8) red[wid * 8 + mycol] = v1;
    BAR_LGKM();
    if (wid == 0 && lane < 8) {
      float tot = 0.f;
#pragma unroll
      for (int w = 0; w < 8; ++w) tot += red[w * 8 + lane];
      const float rs = bfr(r_sel);
      const float sg = bfr(1.0f / (1.0f + __expf(-rs)));
      const float yv = bfr(bfr(tot) * sg);
      const float hv = bflo((unsigned)hrow), hn = bfhi((unsigned)hrow);
      p.out[(size_t)widx * D + colb + lane] = (u16)(cvtpk(hv + bfr(hn + yv), 0.f) & 0xffffu);
    }
  }
  STAMP(13);
  STAMP_OUT();
}

extern "C" void kernel_launch(void* const* d_in, const int* in_sizes, int n_in, void* d_out, int out_size, void* d_ws, size_t ws_size, hipStream_t stream) {
  Params p;
  p.x = (const u16*)d_in[0]; p.wq = (const u16*)d_in[1]; p.wk = (const u16*)d_in[2]; p.wv = (const u16*)d_in[3]; p.wo = (const u16*)d_in[4];
  p.wr = (const u16*)d_in[6]; p.wg = (const u16*)d_in[7]; p.wu = (const u16*)d_in[8]; p.wd = (const u16*)d_in[9];
  p.out = (u16*)d_out; p.ws = (char*)d_ws;
  (void)hipFuncSetAttribute((const void*)fused_kernel, hipFuncAttributeMaxDynamicSharedMemorySize, LDS_BYTES);
  fused_kernel<<<256, 512, LDS_BYTES, stream>>>(p);
}
```

```cpp
#include <hip/hip_runtime.h>
#include <hip/hip_bf16.h>
#include <stdint.h>

typedef unsigned short u16;
typedef unsigned long long u64;
using bf16x8 = __attribute__((ext_vector_type(8))) short;
using bf16x4 = __attribute__((ext_vector_type(4))) short;
using f32x16 = __attribute__((ext_vector_type(16))) float;
using f32x4 = __attribute__((ext_vector_type(4))) float;
using f32x2 = __attribute__((ext_vector_type(2))) float;
using bfx2 = __attribute__((ext_vector_type(2))) __bf16;
using u32x4 = __attribute__((ext_vector_type(4))) unsigned;
using u32x2 = __attribute__((ext_vector_type(2))) unsigned;
#define LAS __attribute__((address_space(3)))
#define GAS __attribute__((address_space(1)))
typedef LAS const char* lds_cptr;
typedef LAS char* lds_ptr;
typedef GAS unsigned gu32;
typedef GAS u64 gu64;
#define RLX_AGENT __ATOMIC_RELAXED, __HIP_MEMORY_SCOPE_AGENT
#define SBAR() __builtin_amdgcn_sched_barrier(0)
#define PIN(x) asm volatile("" : "+v"(x))
#define MFMA16(a, b, c) __builtin_amdgcn_mfma_f32_32x32x16_bf16(a, b, c, 0, 0, 0)
#define MFMA8(a, b, c) __builtin_amdgcn_mfma_f32_32x32x8bf16_1k(a, b, c, 0, 0, 0)
#define MFMAS(a, b, c) __builtin_amdgcn_mfma_f32_16x16x32_bf16(a, b, c, 0, 0, 0)
#define DI __device__ __forceinline__
#define AS8(p) __builtin_bit_cast(bf16x8, p)
#define AS4(p) __builtin_bit_cast(bf16x4, p)

constexpr int S = 2048, D = 640, FF = 1728;
constexpr int TILE_B = 2624;
constexpr float QSCALE = 0.22360679774997896f * 1.4426950408889634f;
constexpr float LN_EPS = 1e-5f;
constexpr unsigned SPIN_CAP = 1u << 18;
#define PSLEEP 1
#define OUTNT 0

constexpr size_t WS_EPOCH = 0;
constexpr size_t WS_F1 = 4096;
constexpr size_t WS_F2 = WS_F1 + 4096;
constexpr size_t WS_CAND = WS_F2 + 4096;
constexpr size_t WS_ACT = WS_CAND + 4096;
constexpr size_t WS_ROW = WS_ACT + 8192;
constexpr size_t WS_ST = WS_ROW + 32 * 5120;
constexpr size_t WS_KV = WS_ST + 256 * 1536;
constexpr size_t WS_O = WS_KV + 64 * TILE_B;

constexpr int L_X = 0, L_WKV = 81920, L_WQA = L_WKV + 25664, L_SC = L_WQA + 51200;
constexpr int L_ST1 = 71680, L_CS = L_ST1 + 1024, L_MISC = L_CS + 640;
constexpr int L_WQB = 0, L_QX = 51200;
constexpr int L_WOA = 0;
constexpr int L_FGU = 51200;
constexpr int L_FD = L_FGU + 20480;
constexpr int L_OA = 99328;
constexpr int L_DUM = 148480;
constexpr int L_FL = 150528;
constexpr int L_Q = 151552;
constexpr int L_QB = L_Q + 8 * 1280;
constexpr int L_WOB = 99328;
constexpr int LDS_BYTES = 163840;
constexpr int L_EX = 0, L_ST2 = L_EX + 20480, L_GS = L_ST2 + 2048, L_RED = L_GS + 6144;

struct Params {
  const u16 *x, *wq, *wk, *wv, *wo, *wr, *wg, *wu, *wd;
  u16* out;
  char* ws;
};

DI unsigned cvtpk(float lo, float hi) { return __builtin_bit_cast(unsigned, __builtin_convertvector((f32x2){lo, hi}, bfx2)); }
DI float bflo(unsigned w) { return __uint_as_float(w << 16); }
DI float bfhi(unsigned w) { return __uint_as_float(w & 0xffff0000u); }
DI float bfr(float v) { return bflo(cvtpk(v, 0.f)); }
DI void glds(const void* sbase, unsigned voff, unsigned lds_dst) {
  unsigned sv;
  asm volatile("s_mov_b32 %0, m0\n\ts_mov_b32 m0, %3\n\ts_nop 0\n\tglobal_load_lds_dwordx4 %1, %2\n\ts_mov_b32 m0, %0" : "=&s"(sv) : "v"(voff), "s"(sbase), "s"(lds_dst) : "memory");
}
DI bf16x4 trread(lds_cptr p) { return AS4(__builtin_amdgcn_ds_read_tr16_b64_v4i16((LAS bf16x4*)p)); }
DI bf16x8 cat8(bf16x4 a, bf16x4 b) { return __builtin_shufflevector(a, b, 0, 1, 2, 3, 4, 5, 6, 7); }
DI float dot2(unsigned a, unsigned b, float c) { return __builtin_amdgcn_fdot2_f32_bf16(__builtin_bit_cast(bfx2, a), __builtin_bit_cast(bfx2, b), c, false); }
DI void st_g64(char* p, unsigned lo, unsigned hi) { __hip_atomic_store((gu64*)p, ((u64)hi << 32) | lo, RLX_AGENT); }
DI void st_out(u32x2* p, u32x2 v) { if (OUTNT == 1) __builtin_nontemporal_store(v, p); else if (OUTNT == 2) st_g64((char*)p, v[0], v[1]); else *p = v; }
DI u64 ld_g64(const char* p) { return __hip_atomic_load((gu64*)p, RLX_AGENT); }
template <int CTRL> DI unsigned dppu(unsigned v) { return __builtin_amdgcn_update_dpp(0u, v, CTRL, 0xf, 0xf, true); }
template <int CTRL> DI float dppf(float v) { return __uint_as_float(dppu<CTRL>(__float_as_uint(v))); }
DI float add16(float v) { auto q = __builtin_amdgcn_permlane16_swap(__float_as_uint(v), __float_as_uint(v), false, false); return __uint_as_float(q[0]) + __uint_as_float(q[1]); }
DI float add32(float v) { auto q = __builtin_amdgcn_permlane32_swap(__float_as_uint(v), __float_as_uint(v), false, false); return __uint_as_float(q[0]) + __uint_as_float(q[1]); }
DI float addrow(float v) { v += dppf<0xB1>(v); v += dppf<0x4E>(v); v += dppf<0x141>(v); v += dppf<0x140>(v); return v; }
DI unsigned umax_(unsigned a, unsigned b) { return a > b ? a : b; }
DI unsigned umaxrow(unsigned v) { v = umax_(v, dppu<0xB1>(v)); v = umax_(v, dppu<0x4E>(v)); v = umax_(v, dppu<0x141>(v)); v = umax_(v, dppu<0x140>(v)); return v; }
DI unsigned umax16(unsigned v) { auto q = __builtin_amdgcn_permlane16_swap(v, v, false, false); return umax_(q[0], q[1]); }
DI unsigned umax32(unsigned v) { auto q = __builtin_amdgcn_permlane32_swap(v, v, false, false); return umax_(q[0], q[1]); }
DI float rs32(float a, float b) { auto q = __builtin_amdgcn_permlane32_swap(__float_as_uint(a), __float_as_uint(b), false, false); return __uint_as_float(q[0]) + __uint_as_float(q[1]); }
DI float rs16(float a, float b) { auto q = __builtin_amdgcn_permlane16_swap(__float_as_uint(a), __float_as_uint(b), false, false); return __uint_as_float(q[0]) + __uint_as_float(q[1]); }
DI unsigned ordkey(float f) { unsigned u = __float_as_uint(f); return (u & 0x80000000u) ? ~u : (u | 0x80000000u); }
#define BAR_LGKM() asm volatile("s_waitcnt lgkmcnt(0)\n\ts_barrier" ::: "memory")
#define DRAIN() asm volatile("s_waitcnt vmcnt(0)" ::: "memory")

#define STAMP(i)
#define DSTAMP(i)
#define STAMP_DECL
#define STAMP_OUT()

__global__ __launch_bounds__(512, 2) void fused_kernel(Params p) {
  extern __shared__ __attribute__((aligned(16))) char lds[];
  char* ws = p.ws;
  const int tid = threadIdx.x, lane = tid & 63;
  const int wid = __builtin_amdgcn_readfirstlane(tid >> 6);
  const int c = lane & 15, g = lane >> 4;
  const int r = lane & 31, h = lane >> 5;
  const int mt = wid & 3, kh = wid >> 2;
  const int b = blockIdx.x, xcd = b & 7, slot = b >> 3, rg = xcd * 4 + (slot >> 3), hg = slot & 7;
  const int kvmt = hg >> 1, isV = hg & 1;
  const bool hasG = slot < 27, hasD = slot >= 22;
  const int gsl = xcd * 27 + slot, dsl = xcd * 10 + slot - 22;
  const bool pub = (wid == 0);
  const bool team = (wid < 4);
  const int sw4 = mt;
  const unsigned lds0 = (unsigned)(uintptr_t)lds;
  const lds_cptr L = (lds_cptr)lds;
  const lds_ptr LW = (lds_ptr)lds;
  STAMP_DECL
  STAMP(0);
  const int q4 = c >> 2, p4 = c & 3;
  u32x4 xf[10];
  if (team) {
    const char* xs_ = (const char*)p.x + (size_t)(rg * 64 + kvmt * 16) * 1280 + wid * 320;
    const unsigned vo = c * 1280 + g * 16;
    asm volatile("global_load_dwordx4 %0, %5, %6\n\tglobal_load_dwordx4 %1, %5, %6 offset:64\n\tglobal_load_dwordx4 %2, %5, %6 offset:128\n\t"
                 "global_load_dwordx4 %3, %5, %6 offset:192\n\tglobal_load_dwordx4 %4, %5, %6 offset:256"
                 : "=&v"(xf[0]), "=&v"(xf[1]), "=&v"(xf[2]), "=&v"(xf[3]), "=&v"(xf[4]) : "v"(vo), "s"(xs_) : "memory");
    const char* src = (const char*)(isV ? p.wv : p.wk) + wid * 6400;
#pragma unroll
    for (int i = 0; i < 6; ++i) glds(src + i * 1024, lane * 16, lds0 + L_WKV + wid * 6400 + i * 1024);
    if (lane < 16) glds(src + 6144, lane * 16, lds0 + L_WKV + wid * 6400 + 6144);
  } else {
    const unsigned voff_x = g * 1280 + ((c ^ g ^ (4 * mt)) * 16);
#pragma unroll
    for (int m = 0; m < 4; ++m) {
      const char* src = (const char*)p.x + (size_t)(rg * 64 + 16 * m + 4 * mt) * 1280;
#pragma unroll
      for (int sg = 0; sg < 5; ++sg) glds(src + sg * 256, voff_x, lds0 + L_X + ((4 * m + mt) * 5 + sg) * 1024);
    }
  }
  unsigned voffw[5];
#pragma unroll
  for (int ph = 0; ph < 5; ++ph) { const int ci = ph * 64 + lane, rw = ci / 10; voffw[ph] = rw * 1280 + (ci - rw * 10) * 16; }
  auto dma_wgrp = [&](const u16* w, int row0, unsigned dst) {
    const char* src = (const char*)w + (size_t)row0 * 1280 + hg * 160;
#pragma unroll
    for (int ph = 0; ph < 5; ++ph) glds(src, voffw[ph], dst + ph * 1024);
  };
  SBAR();
  const unsigned tag = *(const __attribute__((address_space(4))) unsigned*)(ws + WS_EPOCH) + 1u;
  const int ro1 = 8 * g + (g & 1) * 4 + q4, ro2 = 8 * g + (1 - (g & 1)) * 4 + q4;
  bf16x8 ones = AS8(((u32x4){0x3f803f80u, 0x3f803f80u, 0x3f803f80u, 0x3f803f80u}));
  PIN(ones);
  auto diag = [&](const f32x4& gr) { return p4 == 0 ? gr[0] : p4 == 1 ? gr[1] : p4 == 2 ? gr[2] : gr[3]; };
  if (team) {
    if (wid == 1) {
      const unsigned nt = ~tag;
      char* st = ws + WS_ST + (size_t)(rg * 8 + hg) * 1536;
      st_g64(st + lane * 8, 0u, nt); st_g64(st + 512 + lane * 8, 0u, nt); st_g64(st + 1024 + lane * 8, 0u, nt);
      char* rw = ws + WS_ROW + (size_t)rg * 5120 + hg * 640;
      st_g64(rw + lane * 8, 0u, nt);
      char* misc = lane < 16 ? rw + 512 + lane * 8 : lane < 20 ? ws + WS_ACT + (size_t)(hasG ? gsl : 0) * 32 + (lane - 16) * 8
                 : lane == 20 ? ws + WS_F2 + rg * 128 + hg * 8 : ws + WS_CAND + rg * 16 + (lane & 1) * 8;
      const bool on = lane < 16 || (lane < 20 && hasG) || lane == 20 || (lane < 23 && hg == 0);
      if (on) st_g64(misc, 0u, nt);
    }
    unsigned k1 = L_WKV + wid * 6400 + (8 * g + q4) * 40 + p4 * 8;
    unsigned pw = L_WKV + wid * 6400 + (isV ? (4 * g * 16 + c) * 4 : (c * 24 + 4 * g) * 4);
    PIN(k1); PIN(pw);
    asm volatile("s_waitcnt vmcnt(0)" : "+v"(xf[0]), "+v"(xf[1]), "+v"(xf[2]), "+v"(xf[3]), "+v"(xf[4]) :: "memory");
    f32x4 ax0 = {}, ax1 = {}, cx0 = {}, cx1 = {}, sm = {}, gr = {};
    bf16x8 wf[5][2];
#pragma unroll
    for (int s = 0; s < 5; ++s)
#pragma unroll
      for (int u = 0; u < 2; ++u) wf[s][u] = cat8(trread(L + k1 + s * 1280 + u * 32), trread(L + k1 + s * 1280 + u * 32 + 160));
#pragma unroll
    for (int s = 0; s < 5; ++s) { sm = MFMAS(ones, AS8(xf[s]), sm); gr = MFMAS(AS8(xf[s]), AS8(xf[s]), gr); }
#pragma unroll
    for (int s = 0; s < 5; ++s) {
      ax0 = MFMAS(wf[s][0], AS8(xf[s]), ax0); ax1 = MFMAS(wf[s][1], AS8(xf[s]), ax1);
      cx0 = MFMAS(wf[s][0], ones, cx0); cx1 = MFMAS(wf[s][1], ones, cx1);
    }
    SBAR();
    const lds_ptr pb = LW + L_WKV + wid * 6400;
    if (g == q4) *(LAS f32x2*)(pb + 1536 + c * 8) = (f32x2){sm[0], diag(gr)};
    if (c == 0) { *(LAS f32x4*)(pb + 1664 + g * 16) = cx0; *(LAS f32x4*)(pb + 1664 + 64 + g * 16) = cx1; }
    if (isV) {
#pragma unroll
      for (int i = 0; i < 4; ++i) *(LAS float*)(LW + pw + i * 64) = ax0[i];
      if (g == 0) {
#pragma unroll
        for (int i = 0; i < 4; ++i) *(LAS float*)(LW + pw + 1024 + i * 64) = ax1[i];
      }
    } else {
      *(LAS f32x4*)(LW + pw) = ax0;
      if (g == 0) *(LAS f32x4*)(LW + pw + 64) = ax1;
    }
  } else {
#pragma unroll
    for (int j = 0; j < 3; ++j) {
      const int idx = mt + 4 * j, gi = (idx & 1) * 5 + (idx >> 1);
      if (idx < 10) dma_wgrp(p.wq, (idx & 1) * 320 + (idx >> 1) * 32, lds0 + L_WQA + gi * 5120);
    }
    asm volatile("s_waitcnt vmcnt(10)" ::: "memory");
  }
  unsigned xa[4];
#pragma unroll
  for (int u = 0; u < 4; ++u) { xa[u] = L_X + (4 * mt + q4) * 5120 + p4 * 256 + ((g ^ p4) * 16) + (((4 * ((u + 2 * kh) & 3)) ^ (c & 12)) * 16) + (g & 1) * 8 + ((u + 10 * kh) >> 2) * 1024; PIN(xa[u]); }
  BAR_LGKM();
  STAMP(1);
#pragma unroll
  for (int s = 0; s < 10; ++s) {
    const unsigned a_ = xa[s & 3] + ((s >> 2) * 1024);
    const u32x2 a = *(const LAS u32x2*)(L + a_), bb = *(const LAS u32x2*)(L + (a_ ^ 8));
    xf[s] = (u32x4){a[0], a[1], bb[0], bb[1]};
  }
  BAR_LGKM();
  STAMP(2);
  float kn2 = 0.f;
  if (pub) {
    const lds_cptr pb = L + L_WKV;
    const int gm = rg * 4 + kvmt;
    char* tb = ws + WS_KV + (size_t)(gm >> 1) * TILE_B;
    auto kv_stats = [&](int pp, float& mean, float& rstd) {
      const f32x2 a = (*(const LAS f32x2*)(pb + 1536 + pp * 8) + *(const LAS f32x2*)(pb + 6400 + 1536 + pp * 8)) +
                      (*(const LAS f32x2*)(pb + 12800 + 1536 + pp * 8) + *(const LAS f32x2*)(pb + 19200 + 1536 + pp * 8));
      mean = a[0] * (1.0f / D);
      rstd = __builtin_amdgcn_rsqf(fmaxf(a[1] * (1.0f / D) - mean * mean, 0.f) + LN_EPS);
    };
    if (!isV) {
      float mean, rstd; kv_stats(c, mean, rstd);
      f32x4 a0 = {}, a1 = {}, c0 = {}, c1 = {};
#pragma unroll
      for (int w = 0; w < 4; ++w) {
        a0 += *(const LAS f32x4*)(pb + w * 6400 + (c * 24 + 4 * g) * 4); a1 += *(const LAS f32x4*)(pb + w * 6400 + (c * 24 + 16) * 4);
        c0 += *(const LAS f32x4*)(pb + w * 6400 + 1664 + g * 16); c1 += *(const LAS f32x4*)(pb + w * 6400 + 1664 + 64);
      }
      const unsigned w0 = cvtpk((a0[0] - mean * c0[0]) * rstd, (a0[1] - mean * c0[1]) * rstd), w1 = cvtpk((a0[2] - mean * c0[2]) * rstd, (a0[3] - mean * c0[3]) * rstd);
      const unsigned w2 = cvtpk((a1[0] - mean * c1[0]) * rstd, (a1[1] - mean * c1[1]) * rstd), w3 = cvtpk((a1[2] - mean * c1[2]) * rstd, (a1[3] - mean * c1[3]) * rstd);
      const int rr = (gm & 1) * 16 + c;
      st_g64(tb + ((g >> 1) * 32 + rr) * 16 + (g & 1) * 8, w0, w1);
      if (g == 0) st_g64(tb + 1024 + rr * 8, w2, w3);
      const float n2 = add32(add16(dot2(w0, w0, dot2(w1, w1, 0.f)))) + dot2(w2, w2, dot2(w3, w3, 0.f));
      kn2 = __uint_as_float(umaxrow(__float_as_uint(n2)));
    } else {
      f32x4 a0 = {}, a1 = {}; float cs0 = 0.f, cs1 = 0.f;
#pragma unroll
      for (int w = 0; w < 4; ++w) {
        a0 += *(const LAS f32x4*)(pb + w * 6400 + (c * 16 + 4 * g) * 4); a1 += *(const LAS f32x4*)(pb + w * 6400 + ((16 + p4) * 16 + 4 * g) * 4);
        cs0 += *(const LAS float*)(pb + w * 6400 + 1664 + c * 4); cs1 += *(const LAS float*)(pb + w * 6400 + 1664 + 64 + p4 * 4);
      }
      float v0[4], v1[4];
#pragma unroll
      for (int i = 0; i < 4; ++i) {
        float mean, rstd; kv_stats(4 * g + i, mean, rstd);
        v0[i] = (a0[i] - mean * cs0) * rstd; v1[i] = (a1[i] - mean * cs1) * rstd;
      }
      char* vb = tb + 1280 + (((gm & 1) * 2 + (g & 1)) * 21) * 16 + (g >> 1) * 8;
      st_g64(vb + c * 16, cvtpk(v0[0], v0[1]), cvtpk(v0[2], v0[3]));
      if (c < 5) st_g64(vb + (16 + c) * 16, c < 4 ? cvtpk(v1[0], v1[1]) : 0x3f803f80u, c < 4 ? cvtpk(v1[2], v1[3]) : 0x3f803f80u);
    }
  } else {
    const int w7 = wid - 1;
    dma_wgrp(p.wq, (w7 & 1) * 320 + (w7 >> 1) * 32 + 160, lds0 + L_WQB + ((w7 & 1) * 5 + (w7 >> 1)) * 5120);
    if (w7 < 3) { const int idx = w7 + 7; dma_wgrp(p.wq, (idx & 1) * 320 + (idx >> 1) * 32 + 160, lds0 + L_WQB + ((idx & 1) * 5 + (idx >> 1)) * 5120); }
  }
  {
    f32x4 sm = {}, gr = {};
#pragma unroll
    for (int s = 0; s < 10; ++s) { sm = MFMAS(ones, AS8(xf[s]), sm); gr = MFMAS(AS8(xf[s]), AS8(xf[s]), gr); }
    if (g == q4) *(LAS f32x2*)(LW + L_ST1 + (wid * 16 + c) * 8) = (f32x2){sm[0], diag(gr)};
  }
  SBAR();
  asm volatile("s_waitcnt vmcnt(5)\n\ts_barrier" ::: "memory");
  if (pub) {
    DRAIN();
    if (lane < 2) st_g64(ws + WS_F1 + b * 16 + lane * 8, lane == 0 ? __float_as_uint(kn2) : ~tag ^ 0x5a5a5a5au, tag);
  }
  STAMP(3);
  const unsigned wA1 = (160 * kh + ro1) * 160 + p4 * 8, wA2 = (160 * kh + ro2) * 160 + p4 * 8;
  f32x4 acc[5] = {}, csa = {}, csb = {};
  bf16x8 kl[8], va[8], vb[8]; bf16x4 kx[8];
  const __amdgpu_buffer_rsrc_t rskv = __builtin_amdgcn_make_buffer_rsrc((void*)(ws + WS_KV), 0, 64 * TILE_B, 0x00020000);
  const unsigned kvb = wid * 8 * TILE_B;
  const unsigned oKA = kvb + lane * 16, oKB = kvb + (h == 0 ? 1024 + r * 8 : 1280 + 20 * 16);
  const unsigned oV = r <= 20 ? kvb + 1280 + (h * 21 + r) * 16 : 0x40000000u;
#define LOAD_KV(i) asm volatile("buffer_load_dwordx4 %0, %4, %7, %8 offen sc1\n\tbuffer_load_dwordx2 %1, %5, %7, %8 offen sc1\n\t" \
      "buffer_load_dwordx4 %2, %6, %7, %8 offen sc1\n\tbuffer_load_dwordx4 %3, %6, %7, %8 offen offset:672 sc1" \
      : "+v"(kl[i]), "+v"(kx[i]), "+v"(va[i]), "+v"(vb[i]) : "v"(oKA), "v"(oKB), "v"(oV), "s"(rskv), "s"((i) * TILE_B) : "memory")
#define LOAD_KV_ALL() do { LOAD_KV(0); LOAD_KV(1); LOAD_KV(2); LOAD_KV(3); LOAD_KV(4); LOAD_KV(5); LOAD_KV(6); LOAD_KV(7); } while (0)
#define RDW(dst, o1, o2, off) dst = cat8(trread(L + (o1) + (off)), trread(L + (o2) + (off)))
#define RDU(buf, s, u) do { \
      if ((u) == 0) RDW(wf[buf][0], a1, a2, (s) * 5120); if ((u) == 1) RDW(wf[buf][1], a1, a2, (s) * 5120 + 32); if ((u) == 2) RDW(wf[buf][2], b1, b2, (s) * 5120); \
      if ((u) == 3) RDW(wf[buf][3], c1, c2, (s) * 5120); if ((u) == 4) RDW(wf[buf][4], c1, c2, (s) * 5120 + 32); \
      if ((u) == 5) RDW(wf[buf][5], d1, d2, (s) * 5120); if ((u) == 6) RDW(wf[buf][6], b1, b2, (s) * 5120 + 64); } while (0)
#define PSTEPS(base, XF, s0, CS, KV) do { \
    const unsigned a1 = (base) + wA1 + 96 * kh, a2 = (base) + wA2 + 96 * kh, b1 = (base) + wA1 + 64, b2 = (base) + wA2 + 64; \
    const unsigned c1 = (base) + wA1 + 96 - 96 * kh, c2 = (base) + wA2 + 96 - 96 * kh, d1 = (base) + wA1 + 32 * mt, d2 = (base) + wA2 + 32 * mt; \
    bf16x8 wf[2][7]; \
    _Pragma("unroll") for (int u = 0; u < ((CS) ? 7 : 5); ++u) RDU(0, 0, u); \
    SBAR(); \
    _Pragma("unroll") for (int s = 0; s < 5; ++s) { \
      const bf16x8 xs = AS8(XF[(s0) + s]); \
      _Pragma("unroll") for (int u = 0; u < ((CS) ? 7 : 5); ++u) { \
        if (u < 5) { acc[u] = MFMAS(wf[s & 1][u], xs, acc[u]); PIN(acc[u]); } \
        else if (u == 5) { csa = MFMAS(wf[s & 1][5], ones, csa); PIN(csa); } \
        else { csb = MFMAS(wf[s & 1][6], ones, csb); PIN(csb); } \
        SBAR(); \
        if (s + 1 < 5) RDU((s + 1) & 1, s + 1, u); \
        if ((KV) && s < 4 && u == 0 && kh == 0) { LOAD_KV(2 * (s & 3)); LOAD_KV(2 * (s & 3) + 1); }     \
        SBAR(); \
      } \
    } } while (0)
  PSTEPS(L_WQA, xf, 0, true, false);
  STAMP(17);
  if (pub) {
    unsigned km = 0;
    for (unsigned spins = 0; spins < SPIN_CAP; ++spins) {
      bool ok = true; km = 0;
#pragma unroll
      for (int i = 0; i < 4; ++i) {
        const u64 a = ld_g64(ws + WS_F1 + (i * 64 + lane) * 16), bb = ld_g64(ws + WS_F1 + (i * 64 + lane) * 16 + 8);
        ok &= ((unsigned)(a >> 32) == tag) & ((unsigned)(bb >> 32) == tag) & ((unsigned)bb == (~tag ^ 0x5a5a5a5au));
        km = umax_(km, (unsigned)a);
      }
      if (__all(ok)) break;
      __builtin_amdgcn_s_sleep(1);
    }
    km = umax32(umax16(umaxrow(km)));
    if (lane == 0) *(LAS unsigned*)(LW + L_MISC) = km;
    if (b == 0 && lane == 0) __hip_atomic_store((gu32*)(ws + WS_EPOCH), tag, RLX_AGENT);
  } else {
    DRAIN();
  }
  BAR_LGKM();
  STAMP(4);
  if (tid < 17) *(LAS unsigned*)(LW + L_FL + tid * 4) = 0u;
#pragma unroll
  for (int i = 0; i < 8; ++i) asm volatile("" : "=v"(kl[i]), "=v"(kx[i]), "=v"(va[i]), "=v"(vb[i]));
  PSTEPS(L_WQB, xf, 5, true, true);
  STAMP(15);
  const bool keep2 = (g >> 1) == kh;
  const lds_ptr exw = LW + mt * 5120 + lane * 16;
  {
    LAS float* csp = (LAS float*)(LW + L_CS);
    if (c == 0) {
      *(LAS f32x4*)(csp + (kh * 5 + mt) * 16 + 4 * g) = csa;
      if (mt == 0) *(LAS f32x4*)(csp + (kh * 5 + 4) * 16 + 4 * g) = csb;
    }
    *(LAS f32x4*)(exw + L_QX + 3072 * (1 - kh)) = acc[3]; *(LAS f32x4*)(exw + L_QX + 3072 * (1 - kh) + 1024) = acc[4];
    if (!keep2) *(LAS f32x4*)(exw + L_QX + 2048) = acc[2];
    const float km2 = *(const LAS float*)(L + L_MISC);
    const f32x2 sa = *(const LAS f32x2*)(L + L_ST1 + (mt * 16 + c) * 8), sb = *(const LAS f32x2*)(L + L_ST1 + ((mt + 4) * 16 + c) * 8);
    const float mean = (sa[0] + sb[0]) * (1.0f / D);
    const float rstd = __builtin_amdgcn_rsqf(fmaxf((sa[1] + sb[1]) * (1.0f / D) - mean * mean, 0.f) + LN_EPS) * QSCALE;
    BAR_LGKM();
    const int ph = mt >> 1, rr = (mt & 1) * 16 + c;
#pragma unroll
    for (int u = 0; u < 3; ++u) {
      const int t = u == 2 ? 2 : u + 3 * kh;
      const f32x4 a = acc[u] + *(const LAS f32x4*)(exw + L_QX + t * 1024);
      const f32x4 cv = *(const LAS f32x4*)(csp + t * 16 + 4 * g) + *(const LAS f32x4*)(csp + (5 + t) * 16 + 4 * g);
      const int n = 16 * t + 4 * g, hh = (n * 205) >> 12, d = n - hh * 20;
      const unsigned dst = L_Q + (hh * 2 + ph) * 1280 + (d < 16 ? ((d >> 3) * 32 + rr) * 16 + (d & 4) * 2 : 1024 + rr * 8);
      const u32x2 qv = (u32x2){cvtpk((a[0] - mean * cv[0]) * rstd, (a[1] - mean * cv[1]) * rstd), cvtpk((a[2] - mean * cv[2]) * rstd, (a[3] - mean * cv[3]) * rstd)};
      if (u < 2 || keep2) *(LAS u32x2*)(LW + dst) = qv;
    }
    BAR_LGKM();
    const u32x4 ql = *(const LAS u32x4*)(L + L_Q + wid * 1280 + lane * 16);
    const u32x2 qh = *(const LAS u32x2*)(L + L_Q + wid * 1280 + 1024 + r * 8);
    float n2 = dot2(ql[0], ql[0], dot2(ql[1], ql[1], 0.f)) + dot2(ql[2], ql[2], dot2(ql[3], ql[3], 0.f));
    if (h == 0) n2 += dot2(qh[0], qh[0], dot2(qh[1], qh[1], 0.f));
    n2 = add32(n2);
    const float M = __builtin_amdgcn_sqrtf(n2 * km2) * 1.0078125f;
    if (h == 0) *(LAS u32x2*)(LW + L_QB + wid * 256 + r * 8) = (u32x2){cvtpk(-M, 0.f), 0u};
  }
  STAMP(16);
  STAMP(5);
  const unsigned oQL = L_Q + lane * 16, oQH = h == 0 ? L_Q + 1024 + r * 8 : L_QB + r * 8;
  const unsigned sQH = h == 0 ? 1280 : 256;
  volatile LAS unsigned* fl = (volatile LAS unsigned*)(LW + L_FL);
#pragma unroll 1
  for (int pass = 0; pass < 2; ++pass) {
    BAR_LGKM();
    if (pass == 0 && kh == 1) LOAD_KV_ALL();
    f32x16 OA = {}, OB = {}, SA, SB;
    u32x4 p0, p1 = {0, 0, 0, 0};
    bf16x8 qcl, qnl; bf16x4 qch, qnh;
    unsigned flv;
    qcl = *(const LAS bf16x8*)(L + oQL + 2 * mt * 1280);
    qch = *(const LAS bf16x4*)(L + oQH + 2 * mt * sQH);
    DRAIN();
    if (kh == 0) __builtin_amdgcn_s_setprio(2); else __builtin_amdgcn_s_setprio(1);
    SBAR();
    DSTAMP(23);
    SA = MFMA8(kx[0], qch, MFMA16(kl[0], qcl, (f32x16){}));
    SBAR();
#define EXG(X, i, P, j) do { X[i] = __builtin_amdgcn_exp2f(X[i]); X[i + 1] = __builtin_amdgcn_exp2f(X[i + 1]); P[j] = cvtpk(X[i], X[i + 1]); \
                             X[i + 2] = __builtin_amdgcn_exp2f(X[i + 2]); X[i + 3] = __builtin_amdgcn_exp2f(X[i + 3]); P[j + 1] = cvtpk(X[i + 2], X[i + 3]); PIN(P); SBAR(); } while (0)
#define STEP(C, N, i, OP, OC, QL, QH, EXTRA) do { \
      N = MFMA16(kl[((i) + 1) & 7], QL, (f32x16){}); PIN(N); SBAR(); \
      EXG(C, 0, p0, 0); \
      OP = MFMA16(vb[((i) + 7) & 7], AS8(p1), OP); PIN(OP); SBAR(); \
      EXG(C, 4, p0, 2); \
      N = MFMA8(kx[((i) + 1) & 7], QH, N); PIN(N); SBAR(); \
      EXTRA; \
      EXG(C, 8, p1, 0); \
      OC = MFMA16(va[i], AS8(p0), OC); PIN(OC); SBAR(); \
      EXG(C, 12, p1, 2); \
    } while (0)
#define WR_O(X, tt) do { const int sl_ = kh * 8 + ((2 * mt + (tt) + 7) & 7); lds_ptr d_ = LW + L_OA + sl_ * 3072 + lane * 16; \
      *(LAS f32x4*)(d_) = (f32x4){X[0], X[1], X[2], X[3]}; *(LAS f32x4*)(d_ + 1024) = (f32x4){X[4], X[5], X[6], X[7]}; \
      *(LAS f32x4*)(d_ + 2048) = (f32x4){X[8], X[9], X[10], X[11]}; asm volatile("" ::: "memory"); \
      fl[sl_] = pass * 16 + (tt) + 1; PIN(X); SBAR(); } while (0)
#define LD_O(X, sl_) do { lds_cptr s_ = L + L_OA + (sl_) * 3072 + lane * 16; \
      const f32x4 a_ = *(const LAS f32x4*)(s_), b_ = *(const LAS f32x4*)(s_ + 1024), c_ = *(const LAS f32x4*)(s_ + 2048); \
      X[0] = a_[0]; X[1] = a_[1]; X[2] = a_[2]; X[3] = a_[3]; X[4] = b_[0]; X[5] = b_[1]; X[6] = b_[2]; X[7] = b_[3]; \
      X[8] = c_[0]; X[9] = c_[1]; X[10] = c_[2]; X[11] = c_[3]; } while (0)
#define RSLOT() (t < 7 ? kh * 8 + ((2 * mt + t + 1) & 7) : ((2 * mt + 6 + kh) & 7))
#define RWANT() ((unsigned)(pass * 16 + t + 1 + ((t == 7) ? kh : 0)))
#define RD_O(X) do { const int sl_ = RSLOT(); flv = fl[sl_]; asm volatile("" ::: "memory"); LD_O(X, sl_); SBAR(); } while (0)
#define CK_O(X) do { if (__builtin_expect(__builtin_amdgcn_readfirstlane(flv) != RWANT(), 0)) { \
        const int sl_ = RSLOT(); \
        while (fl[sl_] != RWANT()) __builtin_amdgcn_s_sleep(1); \
        asm volatile("" ::: "memory"); LD_O(X, sl_); } SBAR(); } while (0)
#define RD_Q() do { const int jq_ = (2 * mt + t + 1) & 7; \
      qnl = *(const LAS bf16x8*)(L + oQL + jq_ * 1280); qnh = *(const LAS bf16x4*)(L + oQH + jq_ * sQH); SBAR(); } while (0)
#define ROUND(OC, OP) do { \
      STEP(SA, SB, 0, OP, OC, qcl, qch, ); \
      STEP(SB, SA, 1, OC, OC, qcl, qch, WR_O(OP, t)); \
      STEP(SA, SB, 2, OC, OC, qcl, qch, ); \
      STEP(SB, SA, 3, OC, OC, qcl, qch, RD_Q()); \
      STEP(SA, SB, 4, OC, OC, qcl, qch, ); \
      STEP(SB, SA, 5, OC, OC, qcl, qch, RD_O(OP)); \
      STEP(SA, SB, 6, OC, OC, qcl, qch, ); \
      STEP(SB, SA, 7, OC, OC, qnl, qnh, CK_O(OP)); \
      qcl = qnl; qch = qnh; \
    } while (0)
#pragma unroll 1
    for (int t = 0; t < 8; ++t) {
      ROUND(OA, OB);
      ++t;
      ROUND(OB, OA);
    }
    OB = MFMA16(vb[7], AS8(p1), OB);
    __builtin_amdgcn_s_setprio(0);
    DSTAMP(14);
    if (kh == 0) {
      WR_O(OB, 8);
      if (pass == 0) {
        int ln = lane; PIN(ln);
        unsigned vw[5];
#pragma unroll
        for (int ph = 0; ph < 5; ++ph) { const int ci = ph * 64 + ln, rw = ci / 10; vw[ph] = rw * 1280 + (ci - rw * 10) * 16; }
#pragma unroll 1
        for (int gj = mt; gj < 10; gj += 4) {
          const char* src = (const char*)p.wo + (size_t)(gj < 5 ? gj * 32 : 320 + (gj - 5) * 32) * 1280 + hg * 160;
#pragma unroll
          for (int ph = 0; ph < 5; ++ph) glds(src, vw[ph], lds0 + L_WOA + gj * 5120 + ph * 1024);
        }
        DSTAMP(20);
        if (hasG) {
#pragma unroll 1
          for (int f = mt; f < 20; f += 4)
            glds((const char*)(f < 10 ? p.wg : p.wu) + (size_t)(f < 10 ? f : f - 10) * (64 * FF * 2) + gsl * 16, ln * (FF * 2), lds0 + L_FGU + f * 1024);
        }
        DSTAMP(21);
        if (hasD) {
#pragma unroll 1
          for (int d = mt; d < 27; d += 4) glds((const char*)p.wd + (size_t)d * (64 * 1280) + dsl * 16, ln * 1280, lds0 + L_FD + d * 1024);
        }
        DSTAMP(22);
      }
      const int sl = 8 + ((2 * mt + 6) & 7);
      while (fl[sl] != (unsigned)(pass * 16 + 8)) __builtin_amdgcn_s_sleep(4);
      asm volatile("" ::: "memory");
      LD_O(OB, sl);
      LD_O(OA, sl - 8);
    }
    STAMP(6);
    float F[12];
#pragma unroll
    for (int i = 0; i < 12; ++i) F[i] = OB[i] + OA[i];
    auto lq = __builtin_amdgcn_permlane32_swap(__float_as_uint(F[8]), __float_as_uint(F[8]), false, false);
    const float l = __uint_as_float(lq[1]);
    if (pass == 0 && __any(!(l >= 5.421010862427522e-20f))) fl[16] = 1u;
    {
      const int j = (2 * mt + 6 + kh) & 7, pos = (j & 1) * 32 + r;
      const float rl = __builtin_amdgcn_rcpf(l);
      char* ob = ws + WS_O + (size_t)rg * 81920 + (size_t)(pos >> 4) * 20480 + (pos & 15) * 16;
      const int col0 = hg * 80 + (j >> 1) * 20;
      auto put = [&](int d, float a0, float a1, float a2, float a3) {
        const int col = col0 + d, oc = col >> 3, gg = oc & 3, half = ((col >> 2) & 1) ^ (gg & 1);
        st_g64(ob + (oc >> 2) * 1024 + gg * 256 + half * 8, cvtpk(a0 * rl, a1 * rl), cvtpk(a2 * rl, a3 * rl));
      };
      put(4 * h, F[0], F[1], F[2], F[3]);
      put(8 + 4 * h, F[4], F[5], F[6], F[7]);
      if (h == 0) put(16, F[8], F[9], F[10], F[11]);
    }
    asm volatile("s_waitcnt vmcnt(0) lgkmcnt(0)\n\ts_barrier" ::: "memory");
    if (__builtin_expect(pass == 1 || fl[16] == 0u, 1)) break;
    {
      LAS float* mx = (LAS float*)(LW + L_OA);
#pragma unroll 1
      for (int j = 0; j < 8; ++j) {
        const bf16x8 ql = *(const LAS bf16x8*)(L + oQL + j * 1280);
        bf16x4 qh = *(const LAS bf16x4*)(L + L_Q + 1024 + r * 8 + j * 1280);
        if (h == 1) qh = (bf16x4){};
        float m = -INFINITY;
#pragma unroll
        for (int i = 0; i < 8; ++i) {
          const f32x16 sc = MFMA8(kx[i], qh, MFMA16(kl[i], ql, (f32x16){}));
#pragma unroll
          for (int e = 0; e < 16; ++e) m = fmaxf(m, sc[e]);
        }
        m = fmaxf(m, __shfl_xor(m, 32));
        if (h == 0) mx[(wid * 8 + j) * 32 + r] = m;
      }
      BAR_LGKM();
      float m = -INFINITY;
#pragma unroll
      for (int w = 0; w < 8; ++w) m = fmaxf(m, mx[(w * 8 + wid) * 32 + r]);
      const float nm = -m, a = bfr(nm), r1 = nm - a, bb = bfr(r1), cc = r1 - bb;
      if (h == 0) *(LAS u32x2*)(LW + L_QB + wid * 256 + r * 8) = (u32x2){cvtpk(a, bb), cvtpk(cc, 0.f)};
    }
  }
  if (tid == 0) st_g64(ws + WS_F2 + rg * 128 + hg * 8, ~tag, tag);
  int lane_t = lane; PIN(lane_t);
  {
  const int lane = lane_t, tid = wid * 64 + lane, c = lane & 15, g = lane >> 4, r = lane & 31;
  const int q4 = c >> 2, p4 = c & 3;
  const unsigned wA1 = (160 * kh + 8 * g + (g & 1) * 4 + q4) * 160 + p4 * 8, wA2 = (160 * kh + 8 * g + (1 - (g & 1)) * 4 + q4) * 160 + p4 * 8;
  const bool keep2 = (g >> 1) == kh;
  const lds_ptr exw = LW + mt * 5120 + lane * 16;
  {
    unsigned vw[5];
#pragma unroll
    for (int ph = 0; ph < 5; ++ph) { const int ci = ph * 64 + lane, rw = ci / 10; vw[ph] = rw * 1280 + (ci - rw * 10) * 16; }
    const char* src = (const char*)p.wo + (size_t)((wid & 1) * 320 + (wid >> 1) * 32 + 160) * 1280 + hg * 160;
#pragma unroll
    for (int ph = 0; ph < 5; ++ph) glds(src, vw[ph], lds0 + L_WOB + ((wid & 1) * 5 + (wid >> 1)) * 5120 + ph * 1024);
    if (wid < 2) {
      src = (const char*)p.wo + (size_t)(wid * 320 + 128 + 160) * 1280 + hg * 160;
#pragma unroll
      for (int ph = 0; ph < 5; ++ph) glds(src, vw[ph], lds0 + L_WOB + (wid * 5 + 4) * 5120 + ph * 1024);
    }
  }
  STAMP(7);
  const int row = rg * 64 + mt * 16 + c;
  int colo[3];
  u32x2 xv[3], wv[3];
#pragma unroll
  for (int u = 0; u < 3; ++u) {
    colo[u] = (u == 2 ? 2 : u + 3 * kh) * 16 + 4 * g;
    xv[u] = *(const u32x2*)(p.x + (size_t)row * D + hg * 80 + colo[u]);
    wv[u] = *(const u32x2*)(p.wr + hg * 80 + colo[u]);
  }
  float sw = 0.f;
#pragma unroll
  for (int i = 0; i < 5; ++i) sw = dot2(((const unsigned*)p.wr)[lane + 64 * i], 0x3f803f80u, sw);
  for (unsigned spins = 0; spins < SPIN_CAP; ++spins) {
    const u64 f = ld_g64(ws + WS_F2 + rg * 128 + (kh * 4 + (lane & 3)) * 8);
    if (__all(f == (((u64)tag << 32) | (unsigned)~tag))) break;
    __builtin_amdgcn_s_sleep(PSLEEP);
  }
  SBAR();
  STAMP(18);
  u32x4 of[10];
  {
    const __amdgpu_buffer_rsrc_t rs = __builtin_amdgcn_make_buffer_rsrc((void*)(ws + WS_O + (size_t)rg * 81920), 0, 81920, 0x00020000);
#pragma unroll
    for (int s = 0; s < 10; ++s) of[s] = __builtin_amdgcn_raw_buffer_load_b128(rs, (mt * 20 + kh * 10) * 1024 + lane * 16 + s * 1024, 0, 16);
  }
  SBAR();
  STAMP(8);
#pragma unroll
  for (int u = 0; u < 5; ++u) acc[u] = (f32x4){};
  PSTEPS(L_WOA, of, 0, false, false);
  BAR_LGKM();
  PSTEPS(L_WOB, of, 5, false, false);
  STAMP(19);
  unsigned hw[3][2];
  {
    *(LAS f32x4*)(exw + L_EX + 3072 * (1 - kh)) = acc[3]; *(LAS f32x4*)(exw + L_EX + 3072 * (1 - kh) + 1024) = acc[4];
    if (!keep2) *(LAS f32x4*)(exw + L_EX + 2048) = acc[2];
    BAR_LGKM();
    float s1 = 0.f, dt = 0.f;
#pragma unroll
    for (int u = 0; u < 3; ++u) {
      const f32x4 a = acc[u] + *(const LAS f32x4*)(exw + L_EX + (u == 2 ? 2 : u + 3 * kh) * 1024);
      const unsigned a01 = cvtpk(a[0], a[1]), a23 = cvtpk(a[2], a[3]);
      hw[u][0] = cvtpk(bflo(xv[u][0]) + bflo(a01), bfhi(xv[u][0]) + bfhi(a01));
      hw[u][1] = cvtpk(bflo(xv[u][1]) + bflo(a23), bfhi(xv[u][1]) + bfhi(a23));
      const unsigned on = (u < 2 || keep2) ? 0x3f803f80u : 0u, w0 = (u < 2 || keep2) ? wv[u][0] : 0u, w1 = (u < 2 || keep2) ? wv[u][1] : 0u;
      if (u == 2 && !keep2) { hw[u][0] = 0u; hw[u][1] = 0u; }
      s1 = dot2(hw[u][0], on, dot2(hw[u][1], on, s1));
      dt = dot2(hw[u][0], w0, dot2(hw[u][1], w1, dt));
    }
    s1 = add32(add16(s1)); dt = add32(add16(dt));
    const float ml = s1 * (1.0f / 40.f);
    float m2 = 0.f;
#pragma unroll
    for (int u = 0; u < 3; ++u) {
      const float d0 = bflo(hw[u][0]) - ml, d1 = bfhi(hw[u][0]) - ml, d2 = bflo(hw[u][1]) - ml, d3 = bfhi(hw[u][1]) - ml;
      const float q = (d0 * d0 + d1 * d1) + (d2 * d2 + d3 * d3);
      m2 += (u < 2 || keep2) ? q : 0.f;
    }
    m2 = add32(add16(m2));
    LAS f32x4* st = (LAS f32x4*)(LW + L_ST2);
    if (g == 0) st[wid * 16 + c] = (f32x4){ml, m2, dt, 0.f};
    BAR_LGKM();
    if (wid == 0) {
      const f32x4 a = st[lane], bb = st[64 + lane];
      const float dm = a[0] - bb[0];
      char* sp = ws + WS_ST + (size_t)(rg * 8 + hg) * 1536 + lane * 8;
      st_g64(sp, __float_as_uint(0.5f * (a[0] + bb[0])), tag); st_g64(sp + 512, __float_as_uint(a[1] + bb[1] + 20.f * dm * dm), tag);
      st_g64(sp + 1024, __float_as_uint(a[2] + bb[2]), tag);
    }
  }
  STAMP(9);
  {
    const char* sp = ws + WS_ST + (size_t)(rg * 8 + wid) * 1536 + lane * 8;
    u64 a, bb, cc;
    for (unsigned spins = 0; spins < SPIN_CAP; ++spins) {
      a = ld_g64(sp); bb = ld_g64(sp + 512); cc = ld_g64(sp + 1024);
      if (__all(((unsigned)(a >> 32) == tag) & ((unsigned)(bb >> 32) == tag) & ((unsigned)(cc >> 32) == tag))) break;
      __builtin_amdgcn_s_sleep(PSLEEP);
    }
    LAS float* gs = (LAS float*)(LW + L_GS) + wid * 192 + lane;
    gs[0] = __uint_as_float((unsigned)a); gs[64] = __uint_as_float((unsigned)bb); gs[128] = __uint_as_float((unsigned)cc);
  }
  sw = add32(add16(addrow(sw)));
  BAR_LGKM();
  STAMP(10);
  float mean_l, rstd_l, r_l; int best;
  {
    const LAS float* gs = (const LAS float*)(L + L_GS) + lane;
    float mu[8], mean = 0.f, M2 = 0.f, dt = 0.f;
#pragma unroll
    for (int i = 0; i < 8; ++i) { mu[i] = gs[i * 192]; mean += mu[i]; M2 += gs[i * 192 + 64]; dt += gs[i * 192 + 128]; }
    mean *= 0.125f;
#pragma unroll
    for (int i = 0; i < 8; ++i) { const float d = mu[i] - mean; M2 += 80.f * d * d; }
    mean_l = mean; rstd_l = __builtin_amdgcn_rsqf(M2 * (1.0f / D) + LN_EPS);
    r_l = rstd_l * (dt - mean * sw);
    const unsigned key = umax32(umax16(umaxrow((ordkey(r_l) & ~63u) | (unsigned)(63 - lane))));
    best = 63 - (int)(key & 63u);
  }
  if (hg == 0 && wid == 0) {
    if (lane == best) st_g64(ws + WS_CAND + rg * 16, __float_as_uint(r_l), tag);
    if (lane == 0) st_g64(ws + WS_CAND + rg * 16 + 8, (unsigned)(rg * 64 + best), tag);
  }
  const int lrow = mt * 16 + c;
  const float mean = __shfl(mean_l, lrow), rstd = __shfl(rstd_l, lrow);
  unsigned ow[3][2];
#pragma unroll
  for (int u = 0; u < 3; ++u) {
    const float h0 = bflo(hw[u][0]), h1 = bfhi(hw[u][0]), h2 = bflo(hw[u][1]), h3 = bfhi(hw[u][1]);
    const unsigned n01 = cvtpk((h0 - mean) * rstd, (h1 - mean) * rstd), n23 = cvtpk((h2 - mean) * rstd, (h3 - mean) * rstd);
    ow[u][0] = cvtpk(h0 + bflo(n01), h1 + bfhi(n01)); ow[u][1] = cvtpk(h2 + bflo(n23), h3 + bfhi(n23));
    if ((u < 2 || keep2) && lrow == best) {
      char* rw = ws + WS_ROW + (size_t)rg * 5120 + (hg * 80 + colo[u]) * 8;
      st_g64(rw, (hw[u][0] & 0xffffu) | (n01 << 16), tag);
      st_g64(rw + 8, (hw[u][0] >> 16) | (n01 & 0xffff0000u), tag);
      st_g64(rw + 16, (hw[u][1] & 0xffffu) | (n23 << 16), tag);
      st_g64(rw + 24, (hw[u][1] >> 16) | (n23 & 0xffff0000u), tag);
    }
  }
  if (wid == 0) {
    u64 a, bb;
    for (unsigned spins = 0; spins < SPIN_CAP; ++spins) {
      a = ld_g64(ws + WS_CAND + r * 16); bb = ld_g64(ws + WS_CAND + r * 16 + 8);
      if (__all(((unsigned)(a >> 32) == tag) & ((unsigned)(bb >> 32) == tag))) break;
      __builtin_amdgcn_s_sleep(PSLEEP);
    }
    const unsigned key = umax16(umaxrow((ordkey(__uint_as_float((unsigned)a)) & ~31u) | (unsigned)(31 - r)));
    if (lane == 31 - (int)(key & 31u)) *(LAS u32x2*)(LW + L_RED + 1024) = (u32x2){(unsigned)a, (unsigned)bb};
  }
  if (lrow != best) {
#pragma unroll
    for (int u = 0; u < 3; ++u)
      if (u < 2 || keep2) st_out((u32x2*)(p.out + (size_t)row * D + hg * 80 + colo[u]), (u32x2){ow[u][0], ow[u][1]});
  }
  STAMP(11);
  BAR_LGKM();
  const u32x2 win = *(const LAS u32x2*)(L + L_RED + 1024);
  const float r_sel = __uint_as_float(win[0]);
  const int widx = (int)win[1] & (S - 1), wrg = widx >> 6;
  if (wrg != rg && lrow == best) {
#pragma unroll
    for (int u = 0; u < 3; ++u)
      if (u < 2 || keep2) st_out((u32x2*)(p.out + (size_t)row * D + hg * 80 + colo[u]), (u32x2){ow[u][0], ow[u][1]});
  }
  STAMP(12);
  if (hasG) {
    const char* rw = ws + WS_ROW + (size_t)wrg * 5120;
    const int k0 = tid, k1 = tid < 128 ? tid + 512 : tid - 128, k2 = tid + 384 < 640 ? tid + 384 : 639;
    u32x4 fw[3];
#pragma unroll
    for (int j = 0; j < 3; ++j) { int e = tid + 512 * j; e = e < 1280 ? e : 1279; fw[j] = *(const LAS u32x4*)(L + L_FGU + e * 16); }
    u64 a0, a1, a2;
    for (unsigned spins = 0; spins < SPIN_CAP; ++spins) {
      a0 = ld_g64(rw + k0 * 8); a1 = ld_g64(rw + k1 * 8); a2 = ld_g64(rw + k2 * 8);
      if (__all(((unsigned)(a0 >> 32) == tag) & ((unsigned)(a1 >> 32) == tag) & ((unsigned)(a2 >> 32) == tag))) break;
      __builtin_amdgcn_s_sleep(PSLEEP);
    }
    const float x0 = bfhi((unsigned)a0), x1 = bfhi((unsigned)a1), x2 = tid < 256 ? bfhi((unsigned)a2) : 0.f;
    const float xg1 = tid < 128 ? x1 : 0.f, xu1 = tid < 128 ? 0.f : x1;
    float v8[8], v4[4];
#pragma unroll
    for (int j = 0; j < 4; ++j) {
      v8[2 * j] = rs32(x0 * bflo(fw[0][j]) + xg1 * bflo(fw[1][j]), xu1 * bflo(fw[1][j]) + x2 * bflo(fw[2][j]));
      v8[2 * j + 1] = rs32(x0 * bfhi(fw[0][j]) + xg1 * bfhi(fw[1][j]), xu1 * bfhi(fw[1][j]) + x2 * bfhi(fw[2][j]));
    }
#pragma unroll
    for (int j = 0; j < 4; ++j) v4[j] = addrow(rs16(v8[j], v8[4 + j]));
    LAS f32x4* red = (LAS f32x4*)(LW + L_RED);
    if (c == 0) red[wid * 4 + g] = (f32x4){v4[0], v4[1], v4[2], v4[3]};
    BAR_LGKM();
    if (wid == 0 && lane < 4) {
      const LAS float* rf = (const LAS float*)(L + L_RED) + 2 * lane;
      float g0 = 0.f, g1 = 0.f, u0 = 0.f, u1 = 0.f;
#pragma unroll
      for (int w = 0; w < 8; ++w) { g0 += rf[w * 16]; g1 += rf[w * 16 + 1]; u0 += rf[w * 16 + 8]; u1 += rf[w * 16 + 9]; }
      g0 = bfr(g0); g1 = bfr(g1); u0 = bfr(u0); u1 = bfr(u1);
      const float s0 = bfr(g0 * __builtin_amdgcn_rcpf(1.0f + __expf(-g0))), s1 = bfr(g1 * __builtin_amdgcn_rcpf(1.0f + __expf(-g1)));
      st_g64(ws + WS_ACT + (size_t)gsl * 32 + lane * 8, cvtpk(s0 * u0, s1 * u1), tag);
    }
  }
  if (hasD) {
    const int tq = tid < 432 ? tid : 431;
    const int colb = dsl * 8;
    u32x4 fw[4];
#pragma unroll
    for (int j = 0; j < 4; ++j) fw[j] = *(const LAS u32x4*)(L + L_FD + (4 * tq + j) * 16);
    u64 hrow, a0, a1;
    for (unsigned spins = 0; spins < SPIN_CAP; ++spins) {
      a0 = ld_g64(ws + WS_ACT + tq * 16); a1 = ld_g64(ws + WS_ACT + tq * 16 + 8);
      hrow = ld_g64(ws + WS_ROW + (size_t)wrg * 5120 + (colb + (lane & 7)) * 8);
      if (__all(((unsigned)(a0 >> 32) == tag) & ((unsigned)(a1 >> 32) == tag) & ((unsigned)(hrow >> 32) == tag))) break;
      __builtin_amdgcn_s_sleep(PSLEEP);
    }
    float av[4] = {bflo((unsigned)a0), bfhi((unsigned)a0), bflo((unsigned)a1), bfhi((unsigned)a1)};
    if (tid >= 432) { av[0] = av[1] = av[2] = av[3] = 0.f; }
    float y[8], v4[4], v2[2];
#pragma unroll
    for (int j = 0; j < 4; ++j) {
      y[2 * j] = (av[0] * bflo(fw[0][j]) + av[1] * bflo(fw[1][j])) + (av[2] * bflo(fw[2][j]) + av[3] * bflo(fw[3][j]));
      y[2 * j + 1] = (av[0] * bfhi(fw[0][j]) + av[1] * bfhi(fw[1][j])) + (av[2] * bfhi(fw[2][j]) + av[3] * bfhi(fw[3][j]));
    }
#pragma unroll
    for (int j = 0; j < 4; ++j) v4[j] = rs32(y[j], y[4 + j]);
#pragma unroll
    for (int j = 0; j < 2; ++j) v2[j] = addrow(rs16(v4[j], v4[2 + j]));
    LAS f32x2* red = (LAS f32x2*)(LW + L_RED + 512);
    if (c == 0) red[wid * 4 + g] = (f32x2){v2[0], v2[1]};
    BAR_LGKM();
    if (wid == 0 && lane < 8) {
      const LAS float* rf = (const LAS float*)(L + L_RED + 512) + lane;
      float tot = 0.f;
#pragma unroll
      for (int w = 0; w < 8; ++w) tot += rf[w * 8];
      const float rs = bfr(r_sel);
      const float sg = bfr(__builtin_amdgcn_rcpf(1.0f + __expf(-rs)));
      const float yv = bfr(bfr(tot) * sg);
      const float hv = bflo((unsigned)hrow), hn = bfhi((unsigned)hrow);
      p.out[(size_t)widx * D + colb + lane] = (u16)(cvtpk(hv + bfr(hn + yv), 0.f) & 0xffffu);
    }
  }
  }
  STAMP(13);
  STAMP_OUT();
}

extern "C" void kernel_launch(void* const* d_in, const int* in_sizes, int n_in, void* d_out, int out_size, void* d_ws, size_t ws_size, hipStream_t stream) {
  Params p;
  p.x = (const u16*)d_in[0]; p.wq = (const u16*)d_in[1]; p.wk = (const u16*)d_in[2]; p.wv = (const u16*)d_in[3]; p.wo = (const u16*)d_in[4];
  p.wr = (const u16*)d_in[6]; p.wg = (const u16*)d_in[7]; p.wu = (const u16*)d_in[8]; p.wd = (const u16*)d_in[9];
  p.out = (u16*)d_out; p.ws = (char*)d_ws;
  (void)hipFuncSetAttribute((const void*)fused_kernel, hipFuncAttributeMaxDynamicSharedMemorySize, LDS_BYTES);
  fused_kernel<<<256, 512, LDS_BYTES, stream>>>(p);
}
```

```cpp
#include <hip/hip_runtime.h>
#include <hip/hip_bf16.h>
#include <stdint.h>

typedef unsigned short u16;
typedef unsigned long long u64;
using bf16x8 = __attribute__((ext_vector_type(8))) short;
using bf16x4 = __attribute__((ext_vector_type(4))) short;
using f32x16 = __attribute__((ext_vector_type(16))) float;
using f32x4 = __attribute__((ext_vector_type(4))) float;
using f32x2 = __attribute__((ext_vector_type(2))) float;
using bfx2 = __attribute__((ext_vector_type(2))) __bf16;
using u32x4 = __attribute__((ext_vector_type(4))) unsigned;
using u32x2 = __attribute__((ext_vector_type(2))) unsigned;
#define LAS __attribute__((address_space(3)))
#define GAS __attribute__((address_space(1)))
typedef LAS const char* lds_cptr;
typedef LAS char* lds_ptr;
typedef GAS unsigned gu32;
typedef GAS u64 gu64;
#define RLX_AGENT __ATOMIC_RELAXED, __HIP_MEMORY_SCOPE_AGENT
#define SBAR() __builtin_amdgcn_sched_barrier(0)
#define PIN(x) asm volatile("" : "+v"(x))
#define MFMA16(a, b, c) __builtin_amdgcn_mfma_f32_32x32x16_bf16(a, b, c, 0, 0, 0)
#define MFMA8(a, b, c) __builtin_amdgcn_mfma_f32_32x32x8bf16_1k(a, b, c, 0, 0, 0)
#define MFMAS(a, b, c) __builtin_amdgcn_mfma_f32_16x16x32_bf16(a, b, c, 0, 0, 0)
#define DI __device__ __forceinline__
#define AS8(p) __builtin_bit_cast(bf16x8, p)
#define AS4(p) __builtin_bit_cast(bf16x4, p)

constexpr int S = 2048, D = 640, FF = 1728;
constexpr int TILE_B = 2624;
constexpr float QSCALE = 0.22360679774997896f * 1.4426950408889634f;
constexpr float LN_EPS = 1e-5f;
constexpr unsigned SPIN_CAP = 1u << 18;
#define PSLEEP 1
#define OUTNT 0

constexpr size_t WS_EPOCH = 0;
constexpr size_t WS_F1 = 4096;
constexpr size_t WS_F2 = WS_F1 + 8 * 4096;
constexpr size_t WS_CAND = WS_F2 + 32 * 1024;
constexpr size_t WS_ACT = WS_CAND + 32 * 256;
constexpr size_t WS_ROW = WS_ACT + 8 * 4096;
constexpr size_t WS_ST = WS_ROW + 32 * 2560;
constexpr size_t WS_KV = WS_ST + 256 * 512;
constexpr size_t WS_O = WS_KV + 64 * TILE_B;
constexpr u64 INVALID = ~0ull;

constexpr int L_X = 0, L_WKV = 81920, L_WQA = L_WKV + 25664, L_SC = L_WQA + 51200;
constexpr int L_ST1 = 71680, L_CS = L_ST1 + 1024, L_MISC = L_CS + 640;
constexpr int L_WQB = 0, L_QX = 51200;
constexpr int L_WOA = 0;
constexpr int L_FGU = 51200;
constexpr int L_FD = L_FGU + 20480;
constexpr int L_OA = 99328;
constexpr int L_DUM = 148480;
constexpr int L_FL = 150528;
constexpr int L_Q = 151552;
constexpr int L_QB = L_Q + 8 * 1280;
constexpr int L_WOB = 99328;
constexpr int LDS_BYTES = 163840;
constexpr int L_EX = 0, L_ST2 = L_EX + 20480, L_GS = L_ST2 + 2048, L_RED = L_GS + 6144;

struct Params {
  const u16 *x, *wq, *wk, *wv, *wo, *wr, *wg, *wu, *wd;
  u16* out;
  char* ws;
};

DI unsigned cvtpk(float lo, float hi) { return __builtin_bit_cast(unsigned, __builtin_convertvector((f32x2){lo, hi}, bfx2)); }
DI float bflo(unsigned w) { return __uint_as_float(w << 16); }
DI float bfhi(unsigned w) { return __uint_as_float(w & 0xffff0000u); }
DI float bfr(float v) { return bflo(cvtpk(v, 0.f)); }
DI void glds(const void* sbase, unsigned voff, unsigned lds_dst) {
  unsigned sv;
  asm volatile("s_mov_b32 %0, m0\n\ts_mov_b32 m0, %3\n\ts_nop 0\n\tglobal_load_lds_dwordx4 %1, %2\n\ts_mov_b32 m0, %0" : "=&s"(sv) : "v"(voff), "s"(sbase), "s"(lds_dst) : "memory");
}
DI bf16x4 trread(lds_cptr p) { return AS4(__builtin_amdgcn_ds_read_tr16_b64_v4i16((LAS bf16x4*)p)); }
DI bf16x8 cat8(bf16x4 a, bf16x4 b) { return __builtin_shufflevector(a, b, 0, 1, 2, 3, 4, 5, 6, 7); }
DI float dot2(unsigned a, unsigned b, float c) { return __builtin_amdgcn_fdot2_f32_bf16(__builtin_bit_cast(bfx2, a), __builtin_bit_cast(bfx2, b), c, false); }
DI void st_g64(char* p, unsigned lo, unsigned hi) { __hip_atomic_store((gu64*)p, ((u64)hi << 32) | lo, RLX_AGENT); }
DI void st_out(u32x2* p, u32x2 v) { if (OUTNT == 1) __builtin_nontemporal_store(v, p); else if (OUTNT == 2) st_g64((char*)p, v[0], v[1]); else *p = v; }
DI void st_val(char* p, unsigned lo, unsigned hi) { st_g64(p, (lo & hi) == 0xffffffffu ? 0xfffffffeu : lo, hi); }
DI u64 ld_g64(const char* p) { return __hip_atomic_load((gu64*)p, RLX_AGENT); }
template <int CTRL> DI unsigned dppu(unsigned v) { return __builtin_amdgcn_update_dpp(0u, v, CTRL, 0xf, 0xf, true); }
template <int CTRL> DI float dppf(float v) { return __uint_as_float(dppu<CTRL>(__float_as_uint(v))); }
DI float add16(float v) { auto q = __builtin_amdgcn_permlane16_swap(__float_as_uint(v), __float_as_uint(v), false, false); return __uint_as_float(q[0]) + __uint_as_float(q[1]); }
DI float add32(float v) { auto q = __builtin_amdgcn_permlane32_swap(__float_as_uint(v), __float_as_uint(v), false, false); return __uint_as_float(q[0]) + __uint_as_float(q[1]); }
DI float addrow(float v) { v += dppf<0xB1>(v); v += dppf<0x4E>(v); v += dppf<0x141>(v); v += dppf<0x140>(v); return v; }
DI unsigned umax_(unsigned a, unsigned b) { return a > b ? a : b; }
DI unsigned umaxrow(unsigned v) { v = umax_(v, dppu<0xB1>(v)); v = umax_(v, dppu<0x4E>(v)); v = umax_(v, dppu<0x141>(v)); v = umax_(v, dppu<0x140>(v)); return v; }
DI unsigned umax16(unsigned v) { auto q = __builtin_amdgcn_permlane16_swap(v, v, false, false); return umax_(q[0], q[1]); }
DI unsigned umax32(unsigned v) { auto q = __builtin_amdgcn_permlane32_swap(v, v, false, false); return umax_(q[0], q[1]); }
DI float rs32(float a, float b) { auto q = __builtin_amdgcn_permlane32_swap(__float_as_uint(a), __float_as_uint(b), false, false); return __uint_as_float(q[0]) + __uint_as_float(q[1]); }
DI float rs16(float a, float b) { auto q = __builtin_amdgcn_permlane16_swap(__float_as_uint(a), __float_as_uint(b), false, false); return __uint_as_float(q[0]) + __uint_as_float(q[1]); }
DI unsigned ordkey(float f) { unsigned u = __float_as_uint(f); return (u & 0x80000000u) ? ~u : (u | 0x80000000u); }
#define BAR_LGKM() asm volatile("s_waitcnt lgkmcnt(0)\n\ts_barrier" ::: "memory")
#define DRAIN() asm volatile("s_waitcnt vmcnt(0)" ::: "memory")

#define STAMP(i)
#define DSTAMP(i)
#define STAMP_DECL
#define STAMP_OUT()

__global__ __launch_bounds__(512, 2) void fused_kernel(Params p) {
  extern __shared__ __attribute__((aligned(16))) char lds[];
  char* ws = p.ws;
  const int tid = threadIdx.x, lane = tid & 63;
  const int wid = __builtin_amdgcn_readfirstlane(tid >> 6);
  const int c = lane & 15, g = lane >> 4;
  const int r = lane & 31, h = lane >> 5;
  const int mt = wid & 3, kh = wid >> 2;
  const int b = blockIdx.x, xcd = b & 7, slot = b >> 3, rg = xcd * 4 + (slot >> 3), hg = slot & 7;
  const int kvmt = hg >> 1, isV = hg & 1;
  const bool hasG = slot < 27, hasD = slot >= 22;
  const int gsl = xcd * 27 + slot, dsl = xcd * 10 + slot - 22;
  const bool pub = (wid == 0);
  const bool team = (wid < 4);
  const int sw4 = mt;
  const unsigned lds0 = (unsigned)(uintptr_t)lds;
  const lds_cptr L = (lds_cptr)lds;
  const lds_ptr LW = (lds_ptr)lds;
  STAMP_DECL
  STAMP(0);
  const int q4 = c >> 2, p4 = c & 3;
  u32x4 xf[10];
  if (team) {
    const char* xs_ = (const char*)p.x + (size_t)(rg * 64 + kvmt * 16) * 1280 + wid * 320;
    const unsigned vo = c * 1280 + g * 16;
    asm volatile("global_load_dwordx4 %0, %5, %6\n\tglobal_load_dwordx4 %1, %5, %6 offset:64\n\tglobal_load_dwordx4 %2, %5, %6 offset:128\n\t"
                 "global_load_dwordx4 %3, %5, %6 offset:192\n\tglobal_load_dwordx4 %4, %5, %6 offset:256"
                 : "=&v"(xf[0]), "=&v"(xf[1]), "=&v"(xf[2]), "=&v"(xf[3]), "=&v"(xf[4]) : "v"(vo), "s"(xs_) : "memory");
    const char* src = (const char*)(isV ? p.wv : p.wk) + wid * 6400;
#pragma unroll
    for (int i = 0; i < 6; ++i) glds(src + i * 1024, lane * 16, lds0 + L_WKV + wid * 6400 + i * 1024);
    if (lane < 16) glds(src + 6144, lane * 16, lds0 + L_WKV + wid * 6400 + 6144);
  } else {
    const unsigned voff_x = g * 1280 + ((c ^ g ^ (4 * mt)) * 16);
#pragma unroll
    for (int m = 0; m < 4; ++m) {
      const char* src = (const char*)p.x + (size_t)(rg * 64 + 16 * m + 4 * mt) * 1280;
#pragma unroll
      for (int sg = 0; sg < 5; ++sg) glds(src + sg * 256, voff_x, lds0 + L_X + ((4 * m + mt) * 5 + sg) * 1024);
    }
  }
  unsigned voffw[5];
#pragma unroll
  for (int ph = 0; ph < 5; ++ph) { const int ci = ph * 64 + lane, rw = ci / 10; voffw[ph] = rw * 1280 + (ci - rw * 10) * 16; }
  auto dma_wgrp = [&](const u16* w, int row0, unsigned dst) {
    const char* src = (const char*)w + (size_t)row0 * 1280 + hg * 160;
#pragma unroll
    for (int ph = 0; ph < 5; ++ph) glds(src, voffw[ph], dst + ph * 1024);
  };
  SBAR();
  const unsigned tag = *(const __attribute__((address_space(4))) unsigned*)(ws + WS_EPOCH) + 1u;
  const int ro1 = 8 * g + (g & 1) * 4 + q4, ro2 = 8 * g + (1 - (g & 1)) * 4 + q4;
  bf16x8 ones = AS8(((u32x4){0x3f803f80u, 0x3f803f80u, 0x3f803f80u, 0x3f803f80u}));
  PIN(ones);
  auto diag = [&](const f32x4& gr) { return p4 == 0 ? gr[0] : p4 == 1 ? gr[1] : p4 == 2 ? gr[2] : gr[3]; };
  if (team) {
    if (wid == 1) {
      st_g64(ws + WS_ST + (size_t)(rg * 8 + hg) * 512 + lane * 8, ~0u, ~0u);
      char* a1 = lane < 40 ? ws + WS_ROW + rg * 2560 + (lane < 20 ? 0 : 1280 - 160) + hg * 160 + lane * 8
               : lane < 48 ? ws + WS_F2 + rg * 1024 + (lane - 40) * 128 + hg * 8 : ws + WS_ACT + ((lane - 48) >> 1) * 4096 + gsl * 16 + (lane & 1) * 8;
      if (lane < 48 || hasG) st_g64(a1, ~0u, ~0u);
      if (hg == 0 && lane < 32) st_g64(ws + WS_CAND + lane * 256 + rg * 8, ~0u, ~0u);
    }
    unsigned k1 = L_WKV + wid * 6400 + (8 * g + q4) * 40 + p4 * 8;
    unsigned pw = L_WKV + wid * 6400 + (isV ? (4 * g * 16 + c) * 4 : (c * 24 + 4 * g) * 4);
    PIN(k1); PIN(pw);
    asm volatile("s_waitcnt vmcnt(0)" : "+v"(xf[0]), "+v"(xf[1]), "+v"(xf[2]), "+v"(xf[3]), "+v"(xf[4]) :: "memory");
    f32x4 ax0 = {}, ax1 = {}, cx0 = {}, cx1 = {}, sm = {}, gr = {};
    bf16x8 wf[5][2];
#pragma unroll
    for (int s = 0; s < 5; ++s)
#pragma unroll
      for (int u = 0; u < 2; ++u) wf[s][u] = cat8(trread(L + k1 + s * 1280 + u * 32), trread(L + k1 + s * 1280 + u * 32 + 160));
#pragma unroll
    for (int s = 0; s < 5; ++s) { sm = MFMAS(ones, AS8(xf[s]), sm); gr = MFMAS(AS8(xf[s]), AS8(xf[s]), gr); }
#pragma unroll
    for (int s = 0; s < 5; ++s) {
      ax0 = MFMAS(wf[s][0], AS8(xf[s]), ax0); ax1 = MFMAS(wf[s][1], AS8(xf[s]), ax1);
      cx0 = MFMAS(wf[s][0], ones, cx0); cx1 = MFMAS(wf[s][1], ones, cx1);
    }
    SBAR();
    const lds_ptr pb = LW + L_WKV + wid * 6400;
    if (g == q4) *(LAS f32x2*)(pb + 1536 + c * 8) = (f32x2){sm[0], diag(gr)};
    if (c == 0) { *(LAS f32x4*)(pb + 1664 + g * 16) = cx0; *(LAS f32x4*)(pb + 1664 + 64 + g * 16) = cx1; }
    if (isV) {
#pragma unroll
      for (int i = 0; i < 4; ++i) *(LAS float*)(LW + pw + i * 64) = ax0[i];
      if (g == 0) {
#pragma unroll
        for (int i = 0; i < 4; ++i) *(LAS float*)(LW + pw + 1024 + i * 64) = ax1[i];
      }
    } else {
      *(LAS f32x4*)(LW + pw) = ax0;
      if (g == 0) *(LAS f32x4*)(LW + pw + 64) = ax1;
    }
  } else {
#pragma unroll
    for (int j = 0; j < 3; ++j) {
      const int idx = mt + 4 * j, gi = (idx & 1) * 5 + (idx >> 1);
      if (idx < 10) dma_wgrp(p.wq, (idx & 1) * 320 + (idx >> 1) * 32, lds0 + L_WQA + gi * 5120);
    }
    asm volatile("s_waitcnt vmcnt(10)" ::: "memory");
  }
  unsigned xa[4];
#pragma unroll
  for (int u = 0; u < 4; ++u) { xa[u] = L_X + (4 * mt + q4) * 5120 + p4 * 256 + ((g ^ p4) * 16) + (((4 * ((u + 2 * kh) & 3)) ^ (c & 12)) * 16) + (g & 1) * 8 + ((u + 10 * kh) >> 2) * 1024; PIN(xa[u]); }
  BAR_LGKM();
  STAMP(1);
#pragma unroll
  for (int s = 0; s < 10; ++s) {
    const unsigned a_ = xa[s & 3] + ((s >> 2) * 1024);
    const u32x2 a = *(const LAS u32x2*)(L + a_), bb = *(const LAS u32x2*)(L + (a_ ^ 8));
    xf[s] = (u32x4){a[0], a[1], bb[0], bb[1]};
  }
  BAR_LGKM();
  STAMP(2);
  float kn2 = 0.f;
  if (pub) {
    const lds_cptr pb = L + L_WKV;
    const int gm = rg * 4 + kvmt;
    char* tb = ws + WS_KV + (size_t)(gm >> 1) * TILE_B;
    auto kv_stats = [&](int pp, float& mean, float& rstd) {
      const f32x2 a = (*(const LAS f32x2*)(pb + 1536 + pp * 8) + *(const LAS f32x2*)(pb + 6400 + 1536 + pp * 8)) +
                      (*(const LAS f32x2*)(pb + 12800 + 1536 + pp * 8) + *(const LAS f32x2*)(pb + 19200 + 1536 + pp * 8));
      mean = a[0] * (1.0f / D);
      rstd = __builtin_amdgcn_rsqf(fmaxf(a[1] * (1.0f / D) - mean * mean, 0.f) + LN_EPS);
    };
    if (!isV) {
      float mean, rstd; kv_stats(c, mean, rstd);
      f32x4 a0 = {}, a1 = {}, c0 = {}, c1 = {};
#pragma unroll
      for (int w = 0; w < 4; ++w) {
        a0 += *(const LAS f32x4*)(pb + w * 6400 + (c * 24 + 4 * g) * 4); a1 += *(const LAS f32x4*)(pb + w * 6400 + (c * 24 + 16) * 4);
        c0 += *(const LAS f32x4*)(pb + w * 6400 + 1664 + g * 16); c1 += *(const LAS f32x4*)(pb + w * 6400 + 1664 + 64);
      }
      const unsigned w0 = cvtpk((a0[0] - mean * c0[0]) * rstd, (a0[1] - mean * c0[1]) * rstd), w1 = cvtpk((a0[2] - mean * c0[2]) * rstd, (a0[3] - mean * c0[3]) * rstd);
      const unsigned w2 = cvtpk((a1[0] - mean * c1[0]) * rstd, (a1[1] - mean * c1[1]) * rstd), w3 = cvtpk((a1[2] - mean * c1[2]) * rstd, (a1[3] - mean * c1[3]) * rstd);
      const int rr = (gm & 1) * 16 + c;
      st_g64(tb + ((g >> 1) * 32 + rr) * 16 + (g & 1) * 8, w0, w1);
      if (g == 0) st_g64(tb + 1024 + rr * 8, w2, w3);
      const float n2 = add32(add16(dot2(w0, w0, dot2(w1, w1, 0.f)))) + dot2(w2, w2, dot2(w3, w3, 0.f));
      kn2 = __uint_as_float(umaxrow(__float_as_uint(n2)));
    } else {
      f32x4 a0 = {}, a1 = {}; float cs0 = 0.f, cs1 = 0.f;
#pragma unroll
      for (int w = 0; w < 4; ++w) {
        a0 += *(const LAS f32x4*)(pb + w * 6400 + (c * 16 + 4 * g) * 4); a1 += *(const LAS f32x4*)(pb + w * 6400 + ((16 + p4) * 16 + 4 * g) * 4);
        cs0 += *(const LAS float*)(pb + w * 6400 + 1664 + c * 4); cs1 += *(const LAS float*)(pb + w * 6400 + 1664 + 64 + p4 * 4);
      }
      float v0[4], v1[4];
#pragma unroll
      for (int i = 0; i < 4; ++i) {
        float mean, rstd; kv_stats(4 * g + i, mean, rstd);
        v0[i] = (a0[i] - mean * cs0) * rstd; v1[i] = (a1[i] - mean * cs1) * rstd;
      }
      char* vb = tb + 1280 + (((gm & 1) * 2 + (g & 1)) * 21) * 16 + (g >> 1) * 8;
      st_g64(vb + c * 16, cvtpk(v0[0], v0[1]), cvtpk(v0[2], v0[3]));
      if (c < 5) st_g64(vb + (16 + c) * 16, c < 4 ? cvtpk(v1[0], v1[1]) : 0x3f803f80u, c < 4 ? cvtpk(v1[2], v1[3]) : 0x3f803f80u);
    }
  } else {
    const int w7 = wid - 1;
    dma_wgrp(p.wq, (w7 & 1) * 320 + (w7 >> 1) * 32 + 160, lds0 + L_WQB + ((w7 & 1) * 5 + (w7 >> 1)) * 5120);
    if (w7 < 3) { const int idx = w7 + 7; dma_wgrp(p.wq, (idx & 1) * 320 + (idx >> 1) * 32 + 160, lds0 + L_WQB + ((idx & 1) * 5 + (idx >> 1)) * 5120); }
  }
  {
    f32x4 sm = {}, gr = {};
#pragma unroll
    for (int s = 0; s < 10; ++s) { sm = MFMAS(ones, AS8(xf[s]), sm); gr = MFMAS(AS8(xf[s]), AS8(xf[s]), gr); }
    if (g == q4) *(LAS f32x2*)(LW + L_ST1 + (wid * 16 + c) * 8) = (f32x2){sm[0], diag(gr)};
  }
  SBAR();
  asm volatile("s_waitcnt vmcnt(5)\n\ts_barrier" ::: "memory");
  if (pub) {
    DRAIN();
    if (lane < 16) st_g64(ws + WS_F1 + (lane >> 1) * 4096 + b * 16 + (lane & 1) * 8, (lane & 1) == 0 ? __float_as_uint(kn2) : ~tag ^ 0x5a5a5a5au, tag);
  }
  STAMP(3);
  const unsigned wA1 = (160 * kh + ro1) * 160 + p4 * 8, wA2 = (160 * kh + ro2) * 160 + p4 * 8;
  f32x4 acc[5] = {}, csa = {}, csb = {};
  bf16x8 kl[8], va[8], vb[8]; bf16x4 kx[8];
  const __amdgpu_buffer_rsrc_t rskv = __builtin_amdgcn_make_buffer_rsrc((void*)(ws + WS_KV), 0, 64 * TILE_B, 0x00020000);
  const unsigned kvb = wid * 8 * TILE_B;
  const unsigned oKA = kvb + lane * 16, oKB = kvb + (h == 0 ? 1024 + r * 8 : 1280 + 20 * 16);
  const unsigned oV = r <= 20 ? kvb + 1280 + (h * 21 + r) * 16 : 0x40000000u;
#define LOAD_KV(i) asm volatile("buffer_load_dwordx4 %0, %4, %7, %8 offen sc1\n\tbuffer_load_dwordx2 %1, %5, %7, %8 offen sc1\n\t" \
      "buffer_load_dwordx4 %2, %6, %7, %8 offen sc1\n\tbuffer_load_dwordx4 %3, %6, %7, %8 offen offset:672 sc1" \
      : "+v"(kl[i]), "+v"(kx[i]), "+v"(va[i]), "+v"(vb[i]) : "v"(oKA), "v"(oKB), "v"(oV), "s"(rskv), "s"((i) * TILE_B) : "memory")
#define LOAD_KV_ALL() do { LOAD_KV(0); LOAD_KV(1); LOAD_KV(2); LOAD_KV(3); LOAD_KV(4); LOAD_KV(5); LOAD_KV(6); LOAD_KV(7); } while (0)
#define RDW(dst, o1, o2, off) dst = cat8(trread(L + (o1) + (off)), trread(L + (o2) + (off)))
#define RDU(buf, s, u) do { \
      if ((u) == 0) RDW(wf[buf][0], a1, a2, (s) * 5120); if ((u) == 1) RDW(wf[buf][1], a1, a2, (s) * 5120 + 32); if ((u) == 2) RDW(wf[buf][2], b1, b2, (s) * 5120); \
      if ((u) == 3) RDW(wf[buf][3], c1, c2, (s) * 5120); if ((u) == 4) RDW(wf[buf][4], c1, c2, (s) * 5120 + 32); \
      if ((u) == 5) RDW(wf[buf][5], d1, d2, (s) * 5120); if ((u) == 6) RDW(wf[buf][6], b1, b2, (s) * 5120 + 64); } while (0)
#define PSTEPS(base, XF, s0, CS, KV) do { \
    const unsigned a1 = (base) + wA1 + 96 * kh, a2 = (base) + wA2 + 96 * kh, b1 = (base) + wA1 + 64, b2 = (base) + wA2 + 64; \
    const unsigned c1 = (base) + wA1 + 96 - 96 * kh, c2 = (base) + wA2 + 96 - 96 * kh, d1 = (base) + wA1 + 32 * mt, d2 = (base) + wA2 + 32 * mt; \
    bf16x8 wf[2][7]; \
    _Pragma("unroll") for (int u = 0; u < ((CS) ? 7 : 5); ++u) RDU(0, 0, u); \
    SBAR(); \
    _Pragma("unroll") for (int s = 0; s < 5; ++s) { \
      const bf16x8 xs = AS8(XF[(s0) + s]); \
      _Pragma("unroll") for (int u = 0; u < ((CS) ? 7 : 5); ++u) { \
        if (u < 5) { acc[u] = MFMAS(wf[s & 1][u], xs, acc[u]); PIN(acc[u]); } \
        else if (u == 5) { csa = MFMAS(wf[s & 1][5], ones, csa); PIN(csa); } \
        else { csb = MFMAS(wf[s & 1][6], ones, csb); PIN(csb); } \
        SBAR(); \
        if (s + 1 < 5) RDU((s + 1) & 1, s + 1, u); \
        if ((KV) && s < 4 && u == 0 && kh == 0) { LOAD_KV(2 * (s & 3)); LOAD_KV(2 * (s & 3) + 1); }     \
        SBAR(); \
      } \
    } } while (0)
  PSTEPS(L_WQA, xf, 0, true, false);
  STAMP(17);
  if (pub) {
    unsigned km = 0;
    for (unsigned spins = 0; spins < SPIN_CAP; ++spins) {
      bool ok = true; km = 0;
#pragma unroll
      for (int i = 0; i < 4; ++i) {
        const u64 a = ld_g64(ws + WS_F1 + xcd * 4096 + (i * 64 + lane) * 16), bb = ld_g64(ws + WS_F1 + xcd * 4096 + (i * 64 + lane) * 16 + 8);
        ok &= ((unsigned)(a >> 32) == tag) & ((unsigned)(bb >> 32) == tag) & ((unsigned)bb == (~tag ^ 0x5a5a5a5au));
        km = umax_(km, (unsigned)a);
      }
      if (__all(ok)) break;
      __builtin_amdgcn_s_sleep(1);
    }
    km = umax32(umax16(umaxrow(km)));
    if (lane == 0) *(LAS unsigned*)(LW + L_MISC) = km;
    if (b == 0 && lane == 0) __hip_atomic_store((gu32*)(ws + WS_EPOCH), tag, RLX_AGENT);
  } else {
    DRAIN();
  }
  BAR_LGKM();
  STAMP(4);
  if (tid < 17) *(LAS unsigned*)(LW + L_FL + tid * 4) = 0u;
#pragma unroll
  for (int i = 0; i < 8; ++i) asm volatile("" : "=v"(kl[i]), "=v"(kx[i]), "=v"(va[i]), "=v"(vb[i]));
  PSTEPS(L_WQB, xf, 5, true, true);
  STAMP(15);
  const bool keep2 = (g >> 1) == kh;
  const lds_ptr exw = LW + mt * 5120 + lane * 16;
  {
    LAS float* csp = (LAS float*)(LW + L_CS);
    if (c == 0) {
      *(LAS f32x4*)(csp + (kh * 5 + mt) * 16 + 4 * g) = csa;
      if (mt == 0) *(LAS f32x4*)(csp + (kh * 5 + 4) * 16 + 4 * g) = csb;
    }
    *(LAS f32x4*)(exw + L_QX + 3072 * (1 - kh)) = acc[3]; *(LAS f32x4*)(exw + L_QX + 3072 * (1 - kh) + 1024) = acc[4];
    if (!keep2) *(LAS f32x4*)(exw + L_QX + 2048) = acc[2];
    const float km2 = *(const LAS float*)(L + L_MISC);
    const f32x2 sa = *(const LAS f32x2*)(L + L_ST1 + (mt * 16 + c) * 8), sb = *(const LAS f32x2*)(L + L_ST1 + ((mt + 4) * 16 + c) * 8);
    const float mean = (sa[0] + sb[0]) * (1.0f / D);
    const float rstd = __builtin_amdgcn_rsqf(fmaxf((sa[1] + sb[1]) * (1.0f / D) - mean * mean, 0.f) + LN_EPS) * QSCALE;
    BAR_LGKM();
    const int ph = mt >> 1, rr = (mt & 1) * 16 + c;
#pragma unroll
    for (int u = 0; u < 3; ++u) {
      const int t = u == 2 ? 2 : u + 3 * kh;
      const f32x4 a = acc[u] + *(const LAS f32x4*)(exw + L_QX + t * 1024);
      const f32x4 cv = *(const LAS f32x4*)(csp + t * 16 + 4 * g) + *(const LAS f32x4*)(csp + (5 + t) * 16 + 4 * g);
      const int n = 16 * t + 4 * g, hh = (n * 205) >> 12, d = n - hh * 20;
      const unsigned dst = L_Q + (hh * 2 + ph) * 1280 + (d < 16 ? ((d >> 3) * 32 + rr) * 16 + (d & 4) * 2 : 1024 + rr * 8);
      const u32x2 qv = (u32x2){cvtpk((a[0] - mean * cv[0]) * rstd, (a[1] - mean * cv[1]) * rstd), cvtpk((a[2] - mean * cv[2]) * rstd, (a[3] - mean * cv[3]) * rstd)};
      if (u < 2 || keep2) *(LAS u32x2*)(LW + dst) = qv;
    }
    BAR_LGKM();
    const u32x4 ql = *(const LAS u32x4*)(L + L_Q + wid * 1280 + lane * 16);
    const u32x2 qh = *(const LAS u32x2*)(L + L_Q + wid * 1280 + 1024 + r * 8);
    float n2 = dot2(ql[0], ql[0], dot2(ql[1], ql[1], 0.f)) + dot2(ql[2], ql[2], dot2(ql[3], ql[3], 0.f));
    if (h == 0) n2 += dot2(qh[0], qh[0], dot2(qh[1], qh[1], 0.f));
    n2 = add32(n2);
    const float M = __builtin_amdgcn_sqrtf(n2 * km2) * 1.0078125f;
    if (h == 0) *(LAS u32x2*)(LW + L_QB + wid * 256 + r * 8) = (u32x2){cvtpk(-M, 0.f), 0u};
  }
  STAMP(16);
  STAMP(5);
  const unsigned oQL = L_Q + lane * 16, oQH = h == 0 ? L_Q + 1024 + r * 8 : L_QB + r * 8;
  const unsigned sQH = h == 0 ? 1280 : 256;
  volatile LAS unsigned* fl = (volatile LAS unsigned*)(LW + L_FL);
#pragma unroll 1
  for (int pass = 0; pass < 2; ++pass) {
    BAR_LGKM();
    if (pass == 0 && kh == 1) LOAD_KV_ALL();
    f32x16 OA = {}, OB = {}, SA, SB;
    u32x4 p0, p1 = {0, 0, 0, 0};
    bf16x8 qcl, qnl; bf16x4 qch, qnh;
    unsigned flv;
    qcl = *(const LAS bf16x8*)(L + oQL + 2 * mt * 1280);
    qch = *(const LAS bf16x4*)(L + oQH + 2 * mt * sQH);
    DRAIN();
    if (kh == 0) __builtin_amdgcn_s_setprio(2); else __builtin_amdgcn_s_setprio(1);
    SBAR();
    DSTAMP(23);
    SA = MFMA8(kx[0], qch, MFMA16(kl[0], qcl, (f32x16){}));
    SBAR();
#define EXG(X, i, P, j) do { X[i] = __builtin_amdgcn_exp2f(X[i]); X[i + 1] = __builtin_amdgcn_exp2f(X[i + 1]); P[j] = cvtpk(X[i], X[i + 1]); \
                             X[i + 2] = __builtin_amdgcn_exp2f(X[i + 2]); X[i + 3] = __builtin_amdgcn_exp2f(X[i + 3]); P[j + 1] = cvtpk(X[i + 2], X[i + 3]); PIN(P); SBAR(); } while (0)
#define STEP(C, N, i, OP, OC, QL, QH, EXTRA) do { \
      N = MFMA16(kl[((i) + 1) & 7], QL, (f32x16){}); PIN(N); SBAR(); \
      EXG(C, 0, p0, 0); \
      OP = MFMA16(vb[((i) + 7) & 7], AS8(p1), OP); PIN(OP); SBAR(); \
      EXG(C, 4, p0, 2); \
      N = MFMA8(kx[((i) + 1) & 7], QH, N); PIN(N); SBAR(); \
      EXTRA; \
      EXG(C, 8, p1, 0); \
      OC = MFMA16(va[i], AS8(p0), OC); PIN(OC); SBAR(); \
      EXG(C, 12, p1, 2); \
    } while (0)
#define WR_O(X, tt) do { const int sl_ = kh * 8 + ((2 * mt + (tt) + 7) & 7); lds_ptr d_ = LW + L_OA + sl_ * 3072 + lane * 16; \
      *(LAS f32x4*)(d_) = (f32x4){X[0], X[1], X[2], X[3]}; *(LAS f32x4*)(d_ + 1024) = (f32x4){X[4], X[5], X[6], X[7]}; \
      *(LAS f32x4*)(d_ + 2048) = (f32x4){X[8], X[9], X[10], X[11]}; asm volatile("" ::: "memory"); \
      fl[sl_] = pass * 16 + (tt) + 1; PIN(X); SBAR(); } while (0)
#define LD_O(X, sl_) do { lds_cptr s_ = L + L_OA + (sl_) * 3072 + lane * 16; \
      const f32x4 a_ = *(const LAS f32x4*)(s_), b_ = *(const LAS f32x4*)(s_ + 1024), c_ = *(const LAS f32x4*)(s_ + 2048); \
      X[0] = a_[0]; X[1] = a_[1]; X[2] = a_[2]; X[3] = a_[3]; X[4] = b_[0]; X[5] = b_[1]; X[6] = b_[2]; X[7] = b_[3]; \
      X[8] = c_[0]; X[9] = c_[1]; X[10] = c_[2]; X[11] = c_[3]; } while (0)
#define RSLOT() (t < 7 ? kh * 8 + ((2 * mt + t + 1) & 7) : ((2 * mt + 6 + kh) & 7))
#define RWANT() ((unsigned)(pass * 16 + t + 1 + ((t == 7) ? kh : 0)))
#define RD_O(X) do { const int sl_ = RSLOT(); flv = fl[sl_]; asm volatile("" ::: "memory"); LD_O(X, sl_); SBAR(); } while (0)
#define CK_O(X) do { if (__builtin_expect(__builtin_amdgcn_readfirstlane(flv) != RWANT(), 0)) { \
        const int sl_ = RSLOT(); \
        while (fl[sl_] != RWANT()) __builtin_amdgcn_s_sleep(1); \
        asm volatile("" ::: "memory"); LD_O(X, sl_); } SBAR(); } while (0)
#define RD_Q() do { const int jq_ = (2 * mt + t + 1) & 7; \
      qnl = *(const LAS bf16x8*)(L + oQL + jq_ * 1280); qnh = *(const LAS bf16x4*)(L + oQH + jq_ * sQH); SBAR(); } while (0)
#define ROUND(OC, OP) do { \
      STEP(SA, SB, 0, OP, OC, qcl, qch, ); \
      STEP(SB, SA, 1, OC, OC, qcl, qch, WR_O(OP, t)); \
      STEP(SA, SB, 2, OC, OC, qcl, qch, ); \
      STEP(SB, SA, 3, OC, OC, qcl, qch, RD_Q()); \
      STEP(SA, SB, 4, OC, OC, qcl, qch, ); \
      STEP(SB, SA, 5, OC, OC, qcl, qch, RD_O(OP)); \
      STEP(SA, SB, 6, OC, OC, qcl, qch, ); \
      STEP(SB, SA, 7, OC, OC, qnl, qnh, CK_O(OP)); \
      qcl = qnl; qch = qnh; \
    } while (0)
#pragma unroll 1
    for (int t = 0; t < 8; ++t) {
      ROUND(OA, OB);
      ++t;
      ROUND(OB, OA);
    }
    OB = MFMA16(vb[7], AS8(p1), OB);
    __builtin_amdgcn_s_setprio(0);
    DSTAMP(14);
    if (kh == 0) {
      WR_O(OB, 8);
      if (pass == 0) {
        int ln = lane; PIN(ln);
        unsigned vw[5];
#pragma unroll
        for (int ph = 0; ph < 5; ++ph) { const int ci = ph * 64 + ln, rw = ci / 10; vw[ph] = rw * 1280 + (ci - rw * 10) * 16; }
#pragma unroll 1
        for (int gj = mt; gj < 10; gj += 4) {
          const char* src = (const char*)p.wo + (size_t)(gj < 5 ? gj * 32 : 320 + (gj - 5) * 32) * 1280 + hg * 160;
#pragma unroll
          for (int ph = 0; ph < 5; ++ph) glds(src, vw[ph], lds0 + L_WOA + gj * 5120 + ph * 1024);
        }
        DSTAMP(20);
        if (hasG) {
#pragma unroll 1
          for (int f = mt; f < 20; f += 4)
            glds((const char*)(f < 10 ? p.wg : p.wu) + (size_t)(f < 10 ? f : f - 10) * (64 * FF * 2) + gsl * 16, ln * (FF * 2), lds0 + L_FGU + f * 1024);
        }
        DSTAMP(21);
        if (hasD) {
#pragma unroll 1
          for (int d = mt; d < 27; d += 4) glds((const char*)p.wd + (size_t)d * (64 * 1280) + dsl * 16, ln * 1280, lds0 + L_FD + d * 1024);
        }
        DSTAMP(22);
      }
      const int sl = 8 + ((2 * mt + 6) & 7);
      while (fl[sl] != (unsigned)(pass * 16 + 8)) __builtin_amdgcn_s_sleep(4);
      asm volatile("" ::: "memory");
      LD_O(OB, sl);
      LD_O(OA, sl - 8);
    }
    STAMP(6);
    float F[12];
#pragma unroll
    for (int i = 0; i < 12; ++i) F[i] = OB[i] + OA[i];
    auto lq = __builtin_amdgcn_permlane32_swap(__float_as_uint(F[8]), __float_as_uint(F[8]), false, false);
    const float l = __uint_as_float(lq[1]);
    if (pass == 0 && __any(!(l >= 5.421010862427522e-20f))) fl[16] = 1u;
    {
      const int j = (2 * mt + 6 + kh) & 7, pos = (j & 1) * 32 + r;
      const float rl = __builtin_amdgcn_rcpf(l);
      char* ob = ws + WS_O + (size_t)rg * 81920 + (size_t)(pos >> 4) * 20480 + (pos & 15) * 16;
      const int col0 = hg * 80 + (j >> 1) * 20;
      auto put = [&](int d, float a0, float a1, float a2, float a3) {
        const int col = col0 + d, oc = col >> 3, gg = oc & 3, half = ((col >> 2) & 1) ^ (gg & 1);
        st_g64(ob + (oc >> 2) * 1024 + gg * 256 + half * 8, cvtpk(a0 * rl, a1 * rl), cvtpk(a2 * rl, a3 * rl));
      };
      put(4 * h, F[0], F[1], F[2], F[3]);
      put(8 + 4 * h, F[4], F[5], F[6], F[7]);
      if (h == 0) put(16, F[8], F[9], F[10], F[11]);
    }
    asm volatile("s_waitcnt vmcnt(0) lgkmcnt(0)\n\ts_barrier" ::: "memory");
    if (__builtin_expect(pass == 1 || fl[16] == 0u, 1)) break;
    {
      LAS float* mx = (LAS float*)(LW + L_OA);
#pragma unroll 1
      for (int j = 0; j < 8; ++j) {
        const bf16x8 ql = *(const LAS bf16x8*)(L + oQL + j * 1280);
        bf16x4 qh = *(const LAS bf16x4*)(L + L_Q + 1024 + r * 8 + j * 1280);
        if (h == 1) qh = (bf16x4){};
        float m = -INFINITY;
#pragma unroll
        for (int i = 0; i < 8; ++i) {
          const f32x16 sc = MFMA8(kx[i], qh, MFMA16(kl[i], ql, (f32x16){}));
#pragma unroll
          for (int e = 0; e < 16; ++e) m = fmaxf(m, sc[e]);
        }
        m = fmaxf(m, __shfl_xor(m, 32));
        if (h == 0) mx[(wid * 8 + j) * 32 + r] = m;
      }
      BAR_LGKM();
      float m = -INFINITY;
#pragma unroll
      for (int w = 0; w < 8; ++w) m = fmaxf(m, mx[(w * 8 + wid) * 32 + r]);
      const float nm = -m, a = bfr(nm), r1 = nm - a, bb = bfr(r1), cc = r1 - bb;
      if (h == 0) *(LAS u32x2*)(LW + L_QB + wid * 256 + r * 8) = (u32x2){cvtpk(a, bb), cvtpk(cc, 0.f)};
    }
  }
  if (tid < 8) st_g64(ws + WS_F2 + rg * 1024 + tid * 128 + hg * 8, 1u, 0u);
  int lane_t = lane; PIN(lane_t);
  {
  const int lane = lane_t, tid = wid * 64 + lane, c = lane & 15, g = lane >> 4, r = lane & 31;
  const int q4 = c >> 2, p4 = c & 3;
  const unsigned wA1 = (160 * kh + 8 * g + (g & 1) * 4 + q4) * 160 + p4 * 8, wA2 = (160 * kh + 8 * g + (1 - (g & 1)) * 4 + q4) * 160 + p4 * 8;
  const bool keep2 = (g >> 1) == kh;
  const lds_ptr exw = LW + mt * 5120 + lane * 16;
  {
    unsigned vw[5];
#pragma unroll
    for (int ph = 0; ph < 5; ++ph) { const int ci = ph * 64 + lane, rw = ci / 10; vw[ph] = rw * 1280 + (ci - rw * 10) * 16; }
    const char* src = (const char*)p.wo + (size_t)((wid & 1) * 320 + (wid >> 1) * 32 + 160) * 1280 + hg * 160;
#pragma unroll
    for (int ph = 0; ph < 5; ++ph) glds(src, vw[ph], lds0 + L_WOB + ((wid & 1) * 5 + (wid >> 1)) * 5120 + ph * 1024);
    if (wid < 2) {
      src = (const char*)p.wo + (size_t)(wid * 320 + 128 + 160) * 1280 + hg * 160;
#pragma unroll
      for (int ph = 0; ph < 5; ++ph) glds(src, vw[ph], lds0 + L_WOB + (wid * 5 + 4) * 5120 + ph * 1024);
    }
  }
  STAMP(7);
  const int row = rg * 64 + mt * 16 + c;
  int colo[3];
  u32x2 xv[3], wv[3];
#pragma unroll
  for (int u = 0; u < 3; ++u) {
    colo[u] = (u == 2 ? 2 : u + 3 * kh) * 16 + 4 * g;
    xv[u] = *(const u32x2*)(p.x + (size_t)row * D + hg * 80 + colo[u]);
    wv[u] = *(const u32x2*)(p.wr + hg * 80 + colo[u]);
  }
  float sw = 0.f;
#pragma unroll
  for (int i = 0; i < 5; ++i) sw = dot2(((const unsigned*)p.wr)[lane + 64 * i], 0x3f803f80u, sw);
  for (unsigned spins = 0; spins < SPIN_CAP; ++spins) {
    const u64 f = ld_g64(ws + WS_F2 + rg * 1024 + hg * 128 + (kh * 4 + (lane & 3)) * 8);
    if (__all(f != INVALID)) break;
    __builtin_amdgcn_s_sleep(PSLEEP);
  }
  SBAR();
  STAMP(18);
  u32x4 of[10];
  {
    const __amdgpu_buffer_rsrc_t rs = __builtin_amdgcn_make_buffer_rsrc((void*)(ws + WS_O + (size_t)rg * 81920), 0, 81920, 0x00020000);
#pragma unroll
    for (int s = 0; s < 10; ++s) of[s] = __builtin_amdgcn_raw_buffer_load_b128(rs, (mt * 20 + kh * 10) * 1024 + lane * 16 + s * 1024, 0, 16);
  }
  SBAR();
  STAMP(8);
#pragma unroll
  for (int u = 0; u < 5; ++u) acc[u] = (f32x4){};
  PSTEPS(L_WOA, of, 0, false, false);
  BAR_LGKM();
  PSTEPS(L_WOB, of, 5, false, false);
  STAMP(19);
  unsigned hw[3][2];
  {
    *(LAS f32x4*)(exw + L_EX + 3072 * (1 - kh)) = acc[3]; *(LAS f32x4*)(exw + L_EX + 3072 * (1 - kh) + 1024) = acc[4];
    if (!keep2) *(LAS f32x4*)(exw + L_EX + 2048) = acc[2];
    BAR_LGKM();
    float s1 = 0.f, dt = 0.f;
#pragma unroll
    for (int u = 0; u < 3; ++u) {
      const f32x4 a = acc[u] + *(const LAS f32x4*)(exw + L_EX + (u == 2 ? 2 : u + 3 * kh) * 1024);
      const unsigned a01 = cvtpk(a[0], a[1]), a23 = cvtpk(a[2], a[3]);
      hw[u][0] = cvtpk(bflo(xv[u][0]) + bflo(a01), bfhi(xv[u][0]) + bfhi(a01));
      hw[u][1] = cvtpk(bflo(xv[u][1]) + bflo(a23), bfhi(xv[u][1]) + bfhi(a23));
      const unsigned on = (u < 2 || keep2) ? 0x3f803f80u : 0u, w0 = (u < 2 || keep2) ? wv[u][0] : 0u, w1 = (u < 2 || keep2) ? wv[u][1] : 0u;
      if (u == 2 && !keep2) { hw[u][0] = 0u; hw[u][1] = 0u; }
      s1 = dot2(hw[u][0], on, dot2(hw[u][1], on, s1));
      dt = dot2(hw[u][0], w0, dot2(hw[u][1], w1, dt));
    }
    s1 = add32(add16(s1)); dt = add32(add16(dt));
    const float ml = s1 * (1.0f / 40.f);
    float m2 = 0.f;
#pragma unroll
    for (int u = 0; u < 3; ++u) {
      const float d0 = bflo(hw[u][0]) - ml, d1 = bfhi(hw[u][0]) - ml, d2 = bflo(hw[u][1]) - ml, d3 = bfhi(hw[u][1]) - ml;
      const float q = (d0 * d0 + d1 * d1) + (d2 * d2 + d3 * d3);
      m2 += (u < 2 || keep2) ? q : 0.f;
    }
    m2 = add32(add16(m2));
    LAS f32x4* st = (LAS f32x4*)(LW + L_ST2);
    if (g == 0) st[wid * 16 + c] = (f32x4){ml, m2, dt, 0.f};
    BAR_LGKM();
    if (wid == 0) {
      const f32x4 a = st[lane], bb = st[64 + lane];
      const float dm = a[0] - bb[0];
      const unsigned q0 = (__float_as_uint(0.5f * (a[0] + bb[0])) + 0x400u) >> 11, q1 = (__float_as_uint(a[1] + bb[1] + 20.f * dm * dm) + 0x400u) >> 11,
                     q2 = (__float_as_uint(a[2] + bb[2]) + 0x400u) >> 11;
      st_g64(ws + WS_ST + (size_t)(rg * 8 + hg) * 512 + lane * 8, q0 | (q1 << 21), (q1 >> 11) | (q2 << 10));
    }
  }
  STAMP(9);
  {
    const char* sp = ws + WS_ST + (size_t)(rg * 8 + wid) * 512 + lane * 8;
    u64 a;
    for (unsigned spins = 0; spins < SPIN_CAP; ++spins) {
      a = ld_g64(sp);
      if (__all(a != INVALID)) break;
      __builtin_amdgcn_s_sleep(PSLEEP);
    }
    LAS float* gs = (LAS float*)(LW + L_GS) + wid * 192 + lane;
    gs[0] = __uint_as_float((unsigned)a << 11); gs[64] = __uint_as_float(((unsigned)(a >> 21)) << 11); gs[128] = __uint_as_float(((unsigned)(a >> 42)) << 11);
  }
  sw = add32(add16(addrow(sw)));
  BAR_LGKM();
  STAMP(10);
  float mean_l, rstd_l, r_l; int best;
  {
    const LAS float* gs = (const LAS float*)(L + L_GS) + lane;
    float mu[8], mean = 0.f, M2 = 0.f, dt = 0.f;
#pragma unroll
    for (int i = 0; i < 8; ++i) { mu[i] = gs[i * 192]; mean += mu[i]; M2 += gs[i * 192 + 64]; dt += gs[i * 192 + 128]; }
    mean *= 0.125f;
#pragma unroll
    for (int i = 0; i < 8; ++i) { const float d = mu[i] - mean; M2 += 80.f * d * d; }
    mean_l = mean; rstd_l = __builtin_amdgcn_rsqf(M2 * (1.0f / D) + LN_EPS);
    r_l = rstd_l * (dt - mean * sw);
    const unsigned key = umax32(umax16(umaxrow((ordkey(r_l) & ~63u) | (unsigned)(63 - lane))));
    best = 63 - (int)(key & 63u);
  }
  if (hg == 0 && wid == 0) {
    const unsigned ck = (__builtin_amdgcn_readlane(ordkey(r_l), best) & ~2047u) | (unsigned)(2047 - (rg * 64 + best));
    if (lane < 32) st_g64(ws + WS_CAND + lane * 256 + rg * 8, ck, 0u);
  }
  const int lrow = mt * 16 + c;
  const float mean = __shfl(mean_l, lrow), rstd = __shfl(rstd_l, lrow);
  unsigned ow[3][2];
#pragma unroll
  for (int u = 0; u < 3; ++u) {
    const float h0 = bflo(hw[u][0]), h1 = bfhi(hw[u][0]), h2 = bflo(hw[u][1]), h3 = bfhi(hw[u][1]);
    const unsigned n01 = cvtpk((h0 - mean) * rstd, (h1 - mean) * rstd), n23 = cvtpk((h2 - mean) * rstd, (h3 - mean) * rstd);
    ow[u][0] = cvtpk(h0 + bflo(n01), h1 + bfhi(n01)); ow[u][1] = cvtpk(h2 + bflo(n23), h3 + bfhi(n23));
    if ((u < 2 || keep2) && lrow == best) {
      char* rw = ws + WS_ROW + rg * 2560 + (hg * 80 + colo[u]) * 2;
      st_val(rw, n01, n23); st_val(rw + 1280, hw[u][0], hw[u][1]);
    }
  }
  if (wid == 0) {
    u64 a;
    for (unsigned spins = 0; spins < SPIN_CAP; ++spins) {
      a = ld_g64(ws + WS_CAND + (b & 31) * 256 + r * 8);
      if (__all(a != INVALID)) break;
      __builtin_amdgcn_s_sleep(PSLEEP);
    }
    const unsigned key = umax16(umaxrow((unsigned)a));
    if (lane == 0) *(LAS unsigned*)(LW + L_RED + 1024) = key;
  }
  if (lrow != best) {
#pragma unroll
    for (int u = 0; u < 3; ++u)
      if (u < 2 || keep2) st_out((u32x2*)(p.out + (size_t)row * D + hg * 80 + colo[u]), (u32x2){ow[u][0], ow[u][1]});
  }
  STAMP(11);
  BAR_LGKM();
  const unsigned win = *(const LAS unsigned*)(L + L_RED + 1024);
  const unsigned wk_ = (win & ~2047u) | 1024u;
  const float r_sel = __uint_as_float((wk_ & 0x80000000u) ? (wk_ & 0x7fffffffu) : ~wk_);
  const int widx = 2047 - (int)(win & 2047u), wrg = widx >> 6;
  if (wrg != rg && lrow == best) {
#pragma unroll
    for (int u = 0; u < 3; ++u)
      if (u < 2 || keep2) st_out((u32x2*)(p.out + (size_t)row * D + hg * 80 + colo[u]), (u32x2){ow[u][0], ow[u][1]});
  }
  STAMP(12);
  if (hasG) {
    const char* rw = ws + WS_ROW + wrg * 2560;
    const int k0 = tid, k1 = tid < 128 ? tid + 512 : tid - 128, k2 = tid + 384 < 640 ? tid + 384 : 639;
    u32x4 fw[3];
#pragma unroll
    for (int j = 0; j < 3; ++j) { int e = tid + 512 * j; e = e < 1280 ? e : 1279; fw[j] = *(const LAS u32x4*)(L + L_FGU + e * 16); }
    u64 a0, a1, a2;
    for (unsigned spins = 0; spins < SPIN_CAP; ++spins) {
      a0 = ld_g64(rw + (k0 >> 2) * 8); a1 = ld_g64(rw + (k1 >> 2) * 8); a2 = ld_g64(rw + (k2 >> 2) * 8);
      if (__all((a0 != INVALID) & (a1 != INVALID) & (a2 != INVALID))) break;
      __builtin_amdgcn_s_sleep(PSLEEP);
    }
    const int sh = (tid & 3) * 16;
    const float x0 = bflo((unsigned)(a0 >> sh)), x1 = bflo((unsigned)(a1 >> sh)), x2 = tid < 256 ? bflo((unsigned)(a2 >> sh)) : 0.f;
    const float xg1 = tid < 128 ? x1 : 0.f, xu1 = tid < 128 ? 0.f : x1;
    float v8[8], v4[4];
#pragma unroll
    for (int j = 0; j < 4; ++j) {
      v8[2 * j] = rs32(x0 * bflo(fw[0][j]) + xg1 * bflo(fw[1][j]), xu1 * bflo(fw[1][j]) + x2 * bflo(fw[2][j]));
      v8[2 * j + 1] = rs32(x0 * bfhi(fw[0][j]) + xg1 * bfhi(fw[1][j]), xu1 * bfhi(fw[1][j]) + x2 * bfhi(fw[2][j]));
    }
#pragma unroll
    for (int j = 0; j < 4; ++j) v4[j] = addrow(rs16(v8[j], v8[4 + j]));
    LAS f32x4* red = (LAS f32x4*)(LW + L_RED);
    if (c == 0) red[wid * 4 + g] = (f32x4){v4[0], v4[1], v4[2], v4[3]};
    BAR_LGKM();
    if (wid == 0) {
      const LAS float* rf = (const LAS float*)(L + L_RED) + (lane & 7);
      float gv = 0.f, uv = 0.f;
#pragma unroll
      for (int w = 0; w < 8; ++w) { gv += rf[w * 16]; uv += rf[w * 16 + 8]; }
      gv = bfr(gv); uv = bfr(uv);
      const float act = bfr(gv * __builtin_amdgcn_rcpf(1.0f + __expf(-gv))) * uv;
      const unsigned pk = cvtpk(act, dppf<0xB1>(act));
      const unsigned pk2 = dppu<0x4E>(pk);
      if ((lane & 3) == 0) st_val(ws + WS_ACT + (lane >> 3) * 4096 + gsl * 16 + (lane & 4) * 2, pk, pk2);
    }
  }
  if (hasD) {
    const int tq = tid < 432 ? tid : 431;
    const int colb = dsl * 8;
    u32x4 fw[4];
#pragma unroll
    for (int j = 0; j < 4; ++j) fw[j] = *(const LAS u32x4*)(L + L_FD + (4 * tq + j) * 16);
    u64 a0, hn4, h4;
    for (unsigned spins = 0; spins < SPIN_CAP; ++spins) {
      a0 = ld_g64(ws + WS_ACT + xcd * 4096 + tq * 8);
      hn4 = ld_g64(ws + WS_ROW + wrg * 2560 + colb * 2 + (lane & 4) * 2); h4 = ld_g64(ws + WS_ROW + wrg * 2560 + 1280 + colb * 2 + (lane & 4) * 2);
      if (__all((a0 != INVALID) & (hn4 != INVALID) & (h4 != INVALID))) break;
      __builtin_amdgcn_s_sleep(PSLEEP);
    }
    float av[4] = {bflo((unsigned)a0), bfhi((unsigned)a0), bflo((unsigned)(a0 >> 32)), bfhi((unsigned)(a0 >> 32))};
    if (tid >= 432) { av[0] = av[1] = av[2] = av[3] = 0.f; }
    float y[8], v4[4], v2[2];
#pragma unroll
    for (int j = 0; j < 4; ++j) {
      y[2 * j] = (av[0] * bflo(fw[0][j]) + av[1] * bflo(fw[1][j])) + (av[2] * bflo(fw[2][j]) + av[3] * bflo(fw[3][j]));
      y[2 * j + 1] = (av[0] * bfhi(fw[0][j]) + av[1] * bfhi(fw[1][j])) + (av[2] * bfhi(fw[2][j]) + av[3] * bfhi(fw[3][j]));
    }
#pragma unroll
    for (int j = 0; j < 4; ++j) v4[j] = rs32(y[j], y[4 + j]);
#pragma unroll
    for (int j = 0; j < 2; ++j) v2[j] = addrow(rs16(v4[j], v4[2 + j]));
    LAS f32x2* red = (LAS f32x2*)(LW + L_RED + 512);
    if (c == 0) red[wid * 4 + g] = (f32x2){v2[0], v2[1]};
    BAR_LGKM();
    if (wid == 0 && lane < 8) {
      const LAS float* rf = (const LAS float*)(L + L_RED + 512) + lane;
      float tot = 0.f;
#pragma unroll
      for (int w = 0; w < 8; ++w) tot += rf[w * 8];
      const float rs = bfr(r_sel);
      const float sg = bfr(__builtin_amdgcn_rcpf(1.0f + __expf(-rs)));
      const float yv = bfr(bfr(tot) * sg);
      const float hv = bflo((unsigned)(h4 >> ((lane & 3) * 16))), hn = bflo((unsigned)(hn4 >> ((lane & 3) * 16)));
      p.out[(size_t)widx * D + colb + lane] = (u16)(cvtpk(hv + bfr(hn + yv), 0.f) & 0xffffu);
    }
  }
  }
  STAMP(13);
  STAMP_OUT();
}

extern "C" void kernel_launch(void* const* d_in, const int* in_sizes, int n_in, void* d_out, int out_size, void* d_ws, size_t ws_size, hipStream_t stream) {
  Params p;
  p.x = (const u16*)d_in[0]; p.wq = (const u16*)d_in[1]; p.wk = (const u16*)d_in[2]; p.wv = (const u16*)d_in[3]; p.wo = (const u16*)d_in[4];
  p.wr = (const u16*)d_in[6]; p.wg = (const u16*)d_in[7]; p.wu = (const u16*)d_in[8]; p.wd = (const u16*)d_in[9];
  p.out = (u16*)d_out; p.ws = (char*)d_ws;
  (void)hipFuncSetAttribute((const void*)fused_kernel, hipFuncAttributeMaxDynamicSharedMemorySize, LDS_BYTES);
  fused_kernel<<<256, 512, LDS_BYTES, stream>>>(p);
}
```

```cpp
#include <hip/hip_runtime.h>
#include <hip/hip_bf16.h>
#include <stdint.h>

typedef unsigned short u16;
typedef unsigned long long u64;
using bf16x8 = __attribute__((ext_vector_type(8))) short;
using bf16x4 = __attribute__((ext_vector_type(4))) short;
using f32x16 = __attribute__((ext_vector_type(16))) float;
using f32x4 = __attribute__((ext_vector_type(4))) float;
using f32x2 = __attribute__((ext_vector_type(2))) float;
using bfx2 = __attribute__((ext_vector_type(2))) __bf16;
using u32x4 = __attribute__((ext_vector_type(4))) unsigned;
using u32x2 = __attribute__((ext_vector_type(2))) unsigned;
#define LAS __attribute__((address_space(3)))
#define GAS __attribute__((address_space(1)))
typedef LAS const char* lds_cptr;
typedef LAS char* lds_ptr;
typedef GAS unsigned gu32;
typedef GAS u64 gu64;
#define RLX_AGENT __ATOMIC_RELAXED, __HIP_MEMORY_SCOPE_AGENT
#define SBAR() __builtin_amdgcn_sched_barrier(0)
#define PIN(x) asm volatile("" : "+v"(x))
#define MFMA16(a, b, c) __builtin_amdgcn_mfma_f32_32x32x16_bf16(a, b, c, 0, 0, 0)
#define MFMA8(a, b, c) __builtin_amdgcn_mfma_f32_32x32x8bf16_1k(a, b, c, 0, 0, 0)
#define MFMAS(a, b, c) __builtin_amdgcn_mfma_f32_16x16x32_bf16(a, b, c, 0, 0, 0)
#define DI __device__ __forceinline__
#define AS8(p) __builtin_bit_cast(bf16x8, p)
#define AS4(p) __builtin_bit_cast(bf16x4, p)

constexpr int S = 2048, D = 640, FF = 1728;
constexpr int TILE_B = 2624;
constexpr float QSCALE = 0.22360679774997896f * 1.4426950408889634f;
constexpr float LN_EPS = 1e-5f;
constexpr unsigned SPIN_CAP = 1u << 18;
#define PSLEEP 1
#define ADELAY 60
#define ASLEEP 2

constexpr size_t WS_EPOCH = 0;
constexpr size_t WS_F1 = 4096;
constexpr size_t WS_F2 = WS_F1 + 8 * 4096;
constexpr size_t WS_CAND = WS_F2 + 32 * 1024;
constexpr size_t WS_ACT = WS_CAND + 32 * 256;
constexpr size_t WS_ROW = WS_ACT + 8 * 4096;
constexpr size_t WS_ST = WS_ROW + 32 * 2560;
constexpr size_t WS_KV = WS_ST + 256 * 512;
constexpr size_t WS_O = WS_KV + 64 * TILE_B;
constexpr u64 INVALID = ~0ull;

constexpr int L_X = 0, L_WKV = 81920, L_WQA = L_WKV + 25664, L_SC = L_WQA + 51200;
constexpr int L_ST1 = 71680, L_CS = L_ST1 + 1024, L_MISC = L_CS + 640;
constexpr int L_WQB = 0, L_QX = 51200;
constexpr int L_WOA = 0;
constexpr int L_FGU = 51200;
constexpr int L_FD = L_FGU + 20480;
constexpr int L_OA = 99328;
constexpr int L_FL = 150528;
constexpr int L_Q = 151552;
constexpr int L_QB = L_Q + 8 * 1280;
constexpr int L_WOB = 99328;
constexpr int LDS_BYTES = 163840;
constexpr int L_EX = 0, L_ST2 = L_EX + 20480, L_GS = L_ST2 + 2048, L_RED = L_GS + 6144;

struct Params {
  const u16 *x, *wq, *wk, *wv, *wo, *wr, *wg, *wu, *wd;
  u16* out;
  char* ws;
};

DI unsigned cvtpk(float lo, float hi) { return __builtin_bit_cast(unsigned, __builtin_convertvector((f32x2){lo, hi}, bfx2)); }
DI float bflo(unsigned w) { return __uint_as_float(w << 16); }
DI float bfhi(unsigned w) { return __uint_as_float(w & 0xffff0000u); }
DI float bfr(float v) { return bflo(cvtpk(v, 0.f)); }
DI void glds(const void* sbase, unsigned voff, unsigned lds_dst) {
  unsigned sv;
  asm volatile("s_mov_b32 %0, m0\n\ts_mov_b32 m0, %3\n\ts_nop 0\n\tglobal_load_lds_dwordx4 %1, %2\n\ts_mov_b32 m0, %0" : "=&s"(sv) : "v"(voff), "s"(sbase), "s"(lds_dst) : "memory");
}
DI bf16x4 trread(lds_cptr p) { return AS4(__builtin_amdgcn_ds_read_tr16_b64_v4i16((LAS bf16x4*)p)); }
DI bf16x8 cat8(bf16x4 a, bf16x4 b) { return __builtin_shufflevector(a, b, 0, 1, 2, 3, 4, 5, 6, 7); }
DI float dot2(unsigned a, unsigned b, float c) { return __builtin_amdgcn_fdot2_f32_bf16(__builtin_bit_cast(bfx2, a), __builtin_bit_cast(bfx2, b), c, false); }
DI void st_g64(char* p, unsigned lo, unsigned hi) { __hip_atomic_store((gu64*)p, ((u64)hi << 32) | lo, RLX_AGENT); }
DI void st_out(u32x2* p, u32x2 v) { *p = v; }
DI void st_val(char* p, unsigned lo, unsigned hi) { st_g64(p, (lo & hi) == 0xffffffffu ? 0xfffffffeu : lo, hi); }
DI u64 ld_g64(const char* p) { return __hip_atomic_load((gu64*)p, RLX_AGENT); }
template <int CTRL> DI unsigned dppu(unsigned v) { return __builtin_amdgcn_update_dpp(0u, v, CTRL, 0xf, 0xf, true); }
template <int CTRL> DI float dppf(float v) { return __uint_as_float(dppu<CTRL>(__float_as_uint(v))); }
DI float add16(float v) { auto q = __builtin_amdgcn_permlane16_swap(__float_as_uint(v), __float_as_uint(v), false, false); return __uint_as_float(q[0]) + __uint_as_float(q[1]); }
DI float add32(float v) { auto q = __builtin_amdgcn_permlane32_swap(__float_as_uint(v), __float_as_uint(v), false, false); return __uint_as_float(q[0]) + __uint_as_float(q[1]); }
DI float addrow(float v) { v += dppf<0xB1>(v); v += dppf<0x4E>(v); v += dppf<0x141>(v); v += dppf<0x140>(v); return v; }
DI unsigned umax_(unsigned a, unsigned b) { return a > b ? a : b; }
DI unsigned umaxrow(unsigned v) { v = umax_(v, dppu<0xB1>(v)); v = umax_(v, dppu<0x4E>(v)); v = umax_(v, dppu<0x141>(v)); v = umax_(v, dppu<0x140>(v)); return v; }
DI unsigned umax16(unsigned v) { auto q = __builtin_amdgcn_permlane16_swap(v, v, false, false); return umax_(q[0], q[1]); }
DI unsigned umax32(unsigned v) { auto q = __builtin_amdgcn_permlane32_swap(v, v, false, false); return umax_(q[0], q[1]); }
DI float rs32(float a, float b) { auto q = __builtin_amdgcn_permlane32_swap(__float_as_uint(a), __float_as_uint(b), false, false); return __uint_as_float(q[0]) + __uint_as_float(q[1]); }
DI float rs16(float a, float b) { auto q = __builtin_amdgcn_permlane16_swap(__float_as_uint(a), __float_as_uint(b), false, false); return __uint_as_float(q[0]) + __uint_as_float(q[1]); }
DI unsigned ordkey(float f) { unsigned u = __float_as_uint(f); return (u & 0x80000000u) ? ~u : (u | 0x80000000u); }
#define BAR_LGKM() asm volatile("s_waitcnt lgkmcnt(0)\n\ts_barrier" ::: "memory")
#define DRAIN() asm volatile("s_waitcnt vmcnt(0)" ::: "memory")


__global__ __launch_bounds__(512, 2) void fused_kernel(Params p) {
  extern __shared__ __attribute__((aligned(16))) char lds[];
  char* ws = p.ws;
  const int tid = threadIdx.x, lane = tid & 63;
  const int wid = __builtin_amdgcn_readfirstlane(tid >> 6);
  const int c = lane & 15, g = lane >> 4;
  const int r = lane & 31, h = lane >> 5;
  const int mt = wid & 3, kh = wid >> 2;
  const int b = blockIdx.x, xcd = b & 7, slot = b >> 3, rg = xcd * 4 + (slot >> 3), hg = slot & 7;
  const int kvmt = hg >> 1, isV = hg & 1;
  const bool hasG = slot < 27, hasD = slot >= 22;
  const int gsl = xcd * 27 + slot, dsl = xcd * 10 + slot - 22;
  const bool pub = (wid == 0);
  const bool team = (wid < 4);
  const int sw4 = mt;
  const unsigned lds0 = (unsigned)(uintptr_t)lds;
  const lds_cptr L = (lds_cptr)lds;
  const lds_ptr LW = (lds_ptr)lds;
  const int q4 = c >> 2, p4 = c & 3;
  u32x4 xf[10];
  if (team) {
    const char* xs_ = (const char*)p.x + (size_t)(rg * 64 + kvmt * 16) * 1280 + wid * 320;
    const unsigned vo = c * 1280 + g * 16;
    asm volatile("global_load_dwordx4 %0, %5, %6\n\tglobal_load_dwordx4 %1, %5, %6 offset:64\n\tglobal_load_dwordx4 %2, %5, %6 offset:128\n\t"
                 "global_load_dwordx4 %3, %5, %6 offset:192\n\tglobal_load_dwordx4 %4, %5, %6 offset:256"
                 : "=&v"(xf[0]), "=&v"(xf[1]), "=&v"(xf[2]), "=&v"(xf[3]), "=&v"(xf[4]) : "v"(vo), "s"(xs_) : "memory");
    const char* src = (const char*)(isV ? p.wv : p.wk) + wid * 6400;
#pragma unroll
    for (int i = 0; i < 6; ++i) glds(src + i * 1024, lane * 16, lds0 + L_WKV + wid * 6400 + i * 1024);
    if (lane < 16) glds(src + 6144, lane * 16, lds0 + L_WKV + wid * 6400 + 6144);
  } else {
    const unsigned voff_x = g * 1280 + ((c ^ g ^ (4 * mt)) * 16);
#pragma unroll
    for (int m = 0; m < 4; ++m) {
      const char* src = (const char*)p.x + (size_t)(rg * 64 + 16 * m + 4 * mt) * 1280;
#pragma unroll
      for (int sg = 0; sg < 5; ++sg) glds(src + sg * 256, voff_x, lds0 + L_X + ((4 * m + mt) * 5 + sg) * 1024);
    }
  }
  unsigned voffw[5];
#pragma unroll
  for (int ph = 0; ph < 5; ++ph) { const int ci = ph * 64 + lane, rw = ci / 10; voffw[ph] = rw * 1280 + (ci - rw * 10) * 16; }
  auto dma_wgrp = [&](const u16* w, int row0, unsigned dst) {
    const char* src = (const char*)w + (size_t)row0 * 1280 + hg * 160;
#pragma unroll
    for (int ph = 0; ph < 5; ++ph) glds(src, voffw[ph], dst + ph * 1024);
  };
  SBAR();
  unsigned tag = 0;
  if (pub) tag = __hip_atomic_load((gu32*)(ws + WS_EPOCH), RLX_AGENT) + 1u;
  const int ro1 = 8 * g + (g & 1) * 4 + q4, ro2 = 8 * g + (1 - (g & 1)) * 4 + q4;
  bf16x8 ones = AS8(((u32x4){0x3f803f80u, 0x3f803f80u, 0x3f803f80u, 0x3f803f80u}));
  PIN(ones);
  auto diag = [&](const f32x4& gr) { return p4 == 0 ? gr[0] : p4 == 1 ? gr[1] : p4 == 2 ? gr[2] : gr[3]; };
  if (team) {
    if (wid == 1) {
      st_g64(ws + WS_ST + (size_t)(rg * 8 + hg) * 512 + lane * 8, ~0u, ~0u);
      char* a1 = lane < 40 ? ws + WS_ROW + rg * 2560 + (lane < 20 ? 0 : 1280 - 160) + hg * 160 + lane * 8
               : lane < 48 ? ws + WS_F2 + rg * 1024 + (lane - 40) * 128 + hg * 8 : ws + WS_ACT + ((lane - 48) >> 1) * 4096 + gsl * 16 + (lane & 1) * 8;
      if (lane < 48 || hasG) st_g64(a1, ~0u, ~0u);
      if (hg == 0 && lane < 32) st_g64(ws + WS_CAND + lane * 256 + rg * 8, ~0u, ~0u);
    }
    unsigned k1 = L_WKV + wid * 6400 + (8 * g + q4) * 40 + p4 * 8;
    unsigned pw = L_WKV + wid * 6400 + (isV ? (4 * g * 16 + c) * 4 : (c * 24 + 4 * g) * 4);
    PIN(k1); PIN(pw);
    asm volatile("s_waitcnt vmcnt(0)" : "+v"(xf[0]), "+v"(xf[1]), "+v"(xf[2]), "+v"(xf[3]), "+v"(xf[4]) :: "memory");
    f32x4 ax0 = {}, ax1 = {}, cx0 = {}, cx1 = {}, sm = {}, gr = {};
    bf16x8 wf[5][2];
#pragma unroll
    for (int s = 0; s < 5; ++s)
#pragma unroll
      for (int u = 0; u < 2; ++u) wf[s][u] = cat8(trread(L + k1 + s * 1280 + u * 32), trread(L + k1 + s * 1280 + u * 32 + 160));
#pragma unroll
    for (int s = 0; s < 5; ++s) { sm = MFMAS(ones, AS8(xf[s]), sm); gr = MFMAS(AS8(xf[s]), AS8(xf[s]), gr); }
#pragma unroll
    for (int s = 0; s < 5; ++s) {
      ax0 = MFMAS(wf[s][0], AS8(xf[s]), ax0); ax1 = MFMAS(wf[s][1], AS8(xf[s]), ax1);
      cx0 = MFMAS(wf[s][0], ones, cx0); cx1 = MFMAS(wf[s][1], ones, cx1);
    }
    SBAR();
    const lds_ptr pb = LW + L_WKV + wid * 6400;
    if (g == q4) *(LAS f32x2*)(pb + 1536 + c * 8) = (f32x2){sm[0], diag(gr)};
    if (c == 0) { *(LAS f32x4*)(pb + 1664 + g * 16) = cx0; *(LAS f32x4*)(pb + 1664 + 64 + g * 16) = cx1; }
    if (isV) {
#pragma unroll
      for (int i = 0; i < 4; ++i) *(LAS float*)(LW + pw + i * 64) = ax0[i];
      if (g == 0) {
#pragma unroll
        for (int i = 0; i < 4; ++i) *(LAS float*)(LW + pw + 1024 + i * 64) = ax1[i];
      }
    } else {
      *(LAS f32x4*)(LW + pw) = ax0;
      if (g == 0) *(LAS f32x4*)(LW + pw + 64) = ax1;
    }
  } else {
#pragma unroll
    for (int j = 0; j < 3; ++j) {
      const int idx = mt + 4 * j, gi = (idx & 1) * 5 + (idx >> 1);
      if (idx < 10) dma_wgrp(p.wq, (idx & 1) * 320 + (idx >> 1) * 32, lds0 + L_WQA + gi * 5120);
    }
    asm volatile("s_waitcnt vmcnt(10)" ::: "memory");
  }
  unsigned xa[4];
#pragma unroll
  for (int u = 0; u < 4; ++u) { xa[u] = L_X + (4 * mt + q4) * 5120 + p4 * 256 + ((g ^ p4) * 16) + (((4 * ((u + 2 * kh) & 3)) ^ (c & 12)) * 16) + (g & 1) * 8 + ((u + 10 * kh) >> 2) * 1024; PIN(xa[u]); }
  BAR_LGKM();
#pragma unroll
  for (int s = 0; s < 10; ++s) {
    const unsigned a_ = xa[s & 3] + ((s >> 2) * 1024);
    const u32x2 a = *(const LAS u32x2*)(L + a_), bb = *(const LAS u32x2*)(L + (a_ ^ 8));
    xf[s] = (u32x4){a[0], a[1], bb[0], bb[1]};
  }
  BAR_LGKM();
  float kn2 = 0.f;
  if (pub) {
    const lds_cptr pb = L + L_WKV;
    const int gm = rg * 4 + kvmt;
    char* tb = ws + WS_KV + (size_t)(gm >> 1) * TILE_B;
    auto kv_stats = [&](int pp, float& mean, float& rstd) {
      const f32x2 a = (*(const LAS f32x2*)(pb + 1536 + pp * 8) + *(const LAS f32x2*)(pb + 6400 + 1536 + pp * 8)) +
                      (*(const LAS f32x2*)(pb + 12800 + 1536 + pp * 8) + *(const LAS f32x2*)(pb + 19200 + 1536 + pp * 8));
      mean = a[0] * (1.0f / D);
      rstd = __builtin_amdgcn_rsqf(fmaxf(a[1] * (1.0f / D) - mean * mean, 0.f) + LN_EPS);
    };
    if (!isV) {
      float mean, rstd; kv_stats(c, mean, rstd);
      f32x4 a0 = {}, a1 = {}, c0 = {}, c1 = {};
#pragma unroll
      for (int w = 0; w < 4; ++w) {
        a0 += *(const LAS f32x4*)(pb + w * 6400 + (c * 24 + 4 * g) * 4); a1 += *(const LAS f32x4*)(pb + w * 6400 + (c * 24 + 16) * 4);
        c0 += *(const LAS f32x4*)(pb + w * 6400 + 1664 + g * 16); c1 += *(const LAS f32x4*)(pb + w * 6400 + 1664 + 64);
      }
      const unsigned w0 = cvtpk((a0[0] - mean * c0[0]) * rstd, (a0[1] - mean * c0[1]) * rstd), w1 = cvtpk((a0[2] - mean * c0[2]) * rstd, (a0[3] - mean * c0[3]) * rstd);
      const unsigned w2 = cvtpk((a1[0] - mean * c1[0]) * rstd, (a1[1] - mean * c1[1]) * rstd), w3 = cvtpk((a1[2] - mean * c1[2]) * rstd, (a1[3] - mean * c1[3]) * rstd);
      const int rr = (gm & 1) * 16 + c;
      st_g64(tb + ((g >> 1) * 32 + rr) * 16 + (g & 1) * 8, w0, w1);
      if (g == 0) st_g64(tb + 1024 + rr * 8, w2, w3);
      const float n2 = add32(add16(dot2(w0, w0, dot2(w1, w1, 0.f)))) + dot2(w2, w2, dot2(w3, w3, 0.f));
      kn2 = __uint_as_float(umaxrow(__float_as_uint(n2)));
    } else {
      f32x4 a0 = {}, a1 = {}; float cs0 = 0.f, cs1 = 0.f;
#pragma unroll
      for (int w = 0; w < 4; ++w) {
        a0 += *(const LAS f32x4*)(pb + w * 6400 + (c * 16 + 4 * g) * 4); a1 += *(const LAS f32x4*)(pb + w * 6400 + ((16 + p4) * 16 + 4 * g) * 4);
        cs0 += *(const LAS float*)(pb + w * 6400 + 1664 + c * 4); cs1 += *(const LAS float*)(pb + w * 6400 + 1664 + 64 + p4 * 4);
      }
      float v0[4], v1[4];
#pragma unroll
      for (int i = 0; i < 4; ++i) {
        float mean, rstd; kv_stats(4 * g + i, mean, rstd);
        v0[i] = (a0[i] - mean * cs0) * rstd; v1[i] = (a1[i] - mean * cs1) * rstd;
      }
      char* vb = tb + 1280 + (((gm & 1) * 2 + (g & 1)) * 21) * 16 + (g >> 1) * 8;
      st_g64(vb + c * 16, cvtpk(v0[0], v0[1]), cvtpk(v0[2], v0[3]));
      if (c < 5) st_g64(vb + (16 + c) * 16, c < 4 ? cvtpk(v1[0], v1[1]) : 0x3f803f80u, c < 4 ? cvtpk(v1[2], v1[3]) : 0x3f803f80u);
    }
  } else {
    const int w7 = wid - 1;
    dma_wgrp(p.wq, (w7 & 1) * 320 + (w7 >> 1) * 32 + 160, lds0 + L_WQB + ((w7 & 1) * 5 + (w7 >> 1)) * 5120);
    if (w7 < 3) { const int idx = w7 + 7; dma_wgrp(p.wq, (idx & 1) * 320 + (idx >> 1) * 32 + 160, lds0 + L_WQB + ((idx & 1) * 5 + (idx >> 1)) * 5120); }
  }
  {
    f32x4 sm = {}, gr = {};
#pragma unroll
    for (int s = 0; s < 10; ++s) { sm = MFMAS(ones, AS8(xf[s]), sm); gr = MFMAS(AS8(xf[s]), AS8(xf[s]), gr); }
    if (g == q4) *(LAS f32x2*)(LW + L_ST1 + (wid * 16 + c) * 8) = (f32x2){sm[0], diag(gr)};
  }
  SBAR();
  asm volatile("s_waitcnt vmcnt(5)\n\ts_barrier" ::: "memory");
  if (pub) {
    DRAIN();
    if (lane < 16) st_g64(ws + WS_F1 + (lane >> 1) * 4096 + b * 16 + (lane & 1) * 8, (lane & 1) == 0 ? __float_as_uint(kn2) : ~tag ^ 0x5a5a5a5au, tag);
  }
  const unsigned wA1 = (160 * kh + ro1) * 160 + p4 * 8, wA2 = (160 * kh + ro2) * 160 + p4 * 8;
  f32x4 acc[5] = {}, csa = {}, csb = {};
  bf16x8 kl[8], va[8], vb[8]; bf16x4 kx[8];
  const __amdgpu_buffer_rsrc_t rskv = __builtin_amdgcn_make_buffer_rsrc((void*)(ws + WS_KV), 0, 64 * TILE_B, 0x00020000);
  const unsigned kvb = wid * 8 * TILE_B;
  const unsigned oKA = kvb + lane * 16, oKB = kvb + (h == 0 ? 1024 + r * 8 : 1280 + 20 * 16);
  const unsigned oV = r <= 20 ? kvb + 1280 + (h * 21 + r) * 16 : 0x40000000u;
#define LOAD_KV(i) asm volatile("buffer_load_dwordx4 %0, %4, %7, %8 offen sc1\n\tbuffer_load_dwordx2 %1, %5, %7, %8 offen sc1\n\t" \
      "buffer_load_dwordx4 %2, %6, %7, %8 offen sc1\n\tbuffer_load_dwordx4 %3, %6, %7, %8 offen offset:672 sc1" \
      : "+v"(kl[i]), "+v"(kx[i]), "+v"(va[i]), "+v"(vb[i]) : "v"(oKA), "v"(oKB), "v"(oV), "s"(rskv), "s"((i) * TILE_B) : "memory")
#define LOAD_KV_ALL() do { LOAD_KV(0); LOAD_KV(1); LOAD_KV(2); LOAD_KV(3); LOAD_KV(4); LOAD_KV(5); LOAD_KV(6); LOAD_KV(7); } while (0)
#define RDW(dst, o1, o2, off) dst = cat8(trread(L + (o1) + (off)), trread(L + (o2) + (off)))
#define RDU(buf, s, u) do { \
      if ((u) == 0) RDW(wf[buf][0], a1, a2, (s) * 5120); if ((u) == 1) RDW(wf[buf][1], a1, a2, (s) * 5120 + 32); if ((u) == 2) RDW(wf[buf][2], b1, b2, (s) * 5120); \
      if ((u) == 3) RDW(wf[buf][3], c1, c2, (s) * 5120); if ((u) == 4) RDW(wf[buf][4], c1, c2, (s) * 5120 + 32); \
      if ((u) == 5) RDW(wf[buf][5], d1, d2, (s) * 5120); if ((u) == 6) RDW(wf[buf][6], b1, b2, (s) * 5120 + 64); } while (0)
#define PSTEPS(base, XF, s0, CS, KV) do { \
    const unsigned a1 = (base) + wA1 + 96 * kh, a2 = (base) + wA2 + 96 * kh, b1 = (base) + wA1 + 64, b2 = (base) + wA2 + 64; \
    const unsigned c1 = (base) + wA1 + 96 - 96 * kh, c2 = (base) + wA2 + 96 - 96 * kh, d1 = (base) + wA1 + 32 * mt, d2 = (base) + wA2 + 32 * mt; \
    bf16x8 wf[2][7]; \
    _Pragma("unroll") for (int u = 0; u < ((CS) ? 7 : 5); ++u) RDU(0, 0, u); \
    SBAR(); \
    _Pragma("unroll") for (int s = 0; s < 5; ++s) { \
      const bf16x8 xs = AS8(XF[(s0) + s]); \
      _Pragma("unroll") for (int u = 0; u < ((CS) ? 7 : 5); ++u) { \
        if (u < 5) { acc[u] = MFMAS(wf[s & 1][u], xs, acc[u]); PIN(acc[u]); } \
        else if (u == 5) { csa = MFMAS(wf[s & 1][5], ones, csa); PIN(csa); } \
        else { csb = MFMAS(wf[s & 1][6], ones, csb); PIN(csb); } \
        SBAR(); \
        if (s + 1 < 5) RDU((s + 1) & 1, s + 1, u); \
        if ((KV) && s < 4 && u == 0 && kh == 0) { LOAD_KV(2 * (s & 3)); LOAD_KV(2 * (s & 3) + 1); }     \
        SBAR(); \
      } \
    } } while (0)
  PSTEPS(L_WQA, xf, 0, true, false);
  if (pub) {
    unsigned km = 0;
    for (unsigned spins = 0; spins < SPIN_CAP; ++spins) {
      bool ok = true; km = 0;
#pragma unroll
      for (int i = 0; i < 4; ++i) {
        const u64 a = ld_g64(ws + WS_F1 + xcd * 4096 + (i * 64 + lane) * 16), bb = ld_g64(ws + WS_F1 + xcd * 4096 + (i * 64 + lane) * 16 + 8);
        ok &= ((unsigned)(a >> 32) == tag) & ((unsigned)(bb >> 32) == tag) & ((unsigned)bb == (~tag ^ 0x5a5a5a5au));
        km = umax_(km, (unsigned)a);
      }
      if (__all(ok)) break;
      __builtin_amdgcn_s_sleep(1);
    }
    km = umax32(umax16(umaxrow(km)));
    if (lane == 0) *(LAS unsigned*)(LW + L_MISC) = km;
    if (b == 0 && lane == 0) __hip_atomic_store((gu32*)(ws + WS_EPOCH), tag, RLX_AGENT);
  } else {
    DRAIN();
  }
  BAR_LGKM();
  if (tid < 17) *(LAS unsigned*)(LW + L_FL + tid * 4) = 0u;
#pragma unroll
  for (int i = 0; i < 8; ++i) asm volatile("" : "=v"(kl[i]), "=v"(kx[i]), "=v"(va[i]), "=v"(vb[i]));
  PSTEPS(L_WQB, xf, 5, true, true);
  const bool keep2 = (g >> 1) == kh;
  const lds_ptr exw = LW + mt * 5120 + lane * 16;
  {
    LAS float* csp = (LAS float*)(LW + L_CS);
    if (c == 0) {
      *(LAS f32x4*)(csp + (kh * 5 + mt) * 16 + 4 * g) = csa;
      if (mt == 0) *(LAS f32x4*)(csp + (kh * 5 + 4) * 16 + 4 * g) = csb;
    }
    *(LAS f32x4*)(exw + L_QX + 3072 * (1 - kh)) = acc[3]; *(LAS f32x4*)(exw + L_QX + 3072 * (1 - kh) + 1024) = acc[4];
    if (!keep2) *(LAS f32x4*)(exw + L_QX + 2048) = acc[2];
    const float km2 = *(const LAS float*)(L + L_MISC);
    const f32x2 sa = *(const LAS f32x2*)(L + L_ST1 + (mt * 16 + c) * 8), sb = *(const LAS f32x2*)(L + L_ST1 + ((mt + 4) * 16 + c) * 8);
    const float mean = (sa[0] + sb[0]) * (1.0f / D);
    const float rstd = __builtin_amdgcn_rsqf(fmaxf((sa[1] + sb[1]) * (1.0f / D) - mean * mean, 0.f) + LN_EPS) * QSCALE;
    BAR_LGKM();
    const int ph = mt >> 1, rr = (mt & 1) * 16 + c;
#pragma unroll
    for (int u = 0; u < 3; ++u) {
      const int t = u == 2 ? 2 : u + 3 * kh;
      const f32x4 a = acc[u] + *(const LAS f32x4*)(exw + L_QX + t * 1024);
      const f32x4 cv = *(const LAS f32x4*)(csp + t * 16 + 4 * g) + *(const LAS f32x4*)(csp + (5 + t) * 16 + 4 * g);
      const int n = 16 * t + 4 * g, hh = (n * 205) >> 12, d = n - hh * 20;
      const unsigned dst = L_Q + (hh * 2 + ph) * 1280 + (d < 16 ? ((d >> 3) * 32 + rr) * 16 + (d & 4) * 2 : 1024 + rr * 8);
      const u32x2 qv = (u32x2){cvtpk((a[0] - mean * cv[0]) * rstd, (a[1] - mean * cv[1]) * rstd), cvtpk((a[2] - mean * cv[2]) * rstd, (a[3] - mean * cv[3]) * rstd)};
      if (u < 2 || keep2) *(LAS u32x2*)(LW + dst) = qv;
    }
    BAR_LGKM();
    const u32x4 ql = *(const LAS u32x4*)(L + L_Q + wid * 1280 + lane * 16);
    const u32x2 qh = *(const LAS u32x2*)(L + L_Q + wid * 1280 + 1024 + r * 8);
    float n2 = dot2(ql[0], ql[0], dot2(ql[1], ql[1], 0.f)) + dot2(ql[2], ql[2], dot2(ql[3], ql[3], 0.f));
    if (h == 0) n2 += dot2(qh[0], qh[0], dot2(qh[1], qh[1], 0.f));
    n2 = add32(n2);
    const float M = __builtin_amdgcn_sqrtf(n2 * km2) * 1.0078125f;
    if (h == 0) *(LAS u32x2*)(LW + L_QB + wid * 256 + r * 8) = (u32x2){cvtpk(-M, 0.f), 0u};
  }
  const unsigned oQL = L_Q + lane * 16, oQH = h == 0 ? L_Q + 1024 + r * 8 : L_QB + r * 8;
  const unsigned sQH = h == 0 ? 1280 : 256;
  volatile LAS unsigned* fl = (volatile LAS unsigned*)(LW + L_FL);
#pragma unroll 1
  for (int pass = 0; pass < 2; ++pass) {
    BAR_LGKM();
    if (pass == 0 && kh == 1) LOAD_KV_ALL();
    f32x16 OA = {}, OB = {}, SA, SB;
    u32x4 p0, p1 = {0, 0, 0, 0};
    bf16x8 qcl, qnl; bf16x4 qch, qnh;
    unsigned flv;
    qcl = *(const LAS bf16x8*)(L + oQL + 2 * mt * 1280);
    qch = *(const LAS bf16x4*)(L + oQH + 2 * mt * sQH);
    DRAIN();
    if (kh == 0) __builtin_amdgcn_s_setprio(2); else __builtin_amdgcn_s_setprio(1);
    SBAR();
    SA = MFMA8(kx[0], qch, MFMA16(kl[0], qcl, (f32x16){}));
    SBAR();
#define EXG(X, i, P, j) do { X[i] = __builtin_amdgcn_exp2f(X[i]); X[i + 1] = __builtin_amdgcn_exp2f(X[i + 1]); P[j] = cvtpk(X[i], X[i + 1]); \
                             X[i + 2] = __builtin_amdgcn_exp2f(X[i + 2]); X[i + 3] = __builtin_amdgcn_exp2f(X[i + 3]); P[j + 1] = cvtpk(X[i + 2], X[i + 3]); PIN(P); SBAR(); } while (0)
#define STEP(C, N, i, OP, OC, QL, QH, EXTRA) do { \
      N = MFMA16(kl[((i) + 1) & 7], QL, (f32x16){}); PIN(N); SBAR(); \
      EXG(C, 0, p0, 0); \
      OP = MFMA16(vb[((i) + 7) & 7], AS8(p1), OP); PIN(OP); SBAR(); \
      EXG(C, 4, p0, 2); \
      N = MFMA8(kx[((i) + 1) & 7], QH, N); PIN(N); SBAR(); \
      EXTRA; \
      EXG(C, 8, p1, 0); \
      OC = MFMA16(va[i], AS8(p0), OC); PIN(OC); SBAR(); \
      EXG(C, 12, p1, 2); \
    } while (0)
#define WR_O(X, tt) do { const int sl_ = kh * 8 + ((2 * mt + (tt) + 7) & 7); lds_ptr d_ = LW + L_OA + sl_ * 3072 + lane * 16; \
      *(LAS f32x4*)(d_) = (f32x4){X[0], X[1], X[2], X[3]}; *(LAS f32x4*)(d_ + 1024) = (f32x4){X[4], X[5], X[6], X[7]}; \
      *(LAS f32x4*)(d_ + 2048) = (f32x4){X[8], X[9], X[10], X[11]}; asm volatile("" ::: "memory"); \
      fl[sl_] = pass * 16 + (tt) + 1; PIN(X); SBAR(); } while (0)
#define LD_O(X, sl_) do { lds_cptr s_ = L + L_OA + (sl_) * 3072 + lane * 16; \
      const f32x4 a_ = *(const LAS f32x4*)(s_), b_ = *(const LAS f32x4*)(s_ + 1024), c_ = *(const LAS f32x4*)(s_ + 2048); \
      X[0] = a_[0]; X[1] = a_[1]; X[2] = a_[2]; X[3] = a_[3]; X[4] = b_[0]; X[5] = b_[1]; X[6] = b_[2]; X[7] = b_[3]; \
      X[8] = c_[0]; X[9] = c_[1]; X[10] = c_[2]; X[11] = c_[3]; } while (0)
#define RSLOT() (t < 7 ? kh * 8 + ((2 * mt + t + 1) & 7) : ((2 * mt + 6 + kh) & 7))
#define RWANT() ((unsigned)(pass * 16 + t + 1 + ((t == 7) ? kh : 0)))
#define RD_O(X) do { const int sl_ = RSLOT(); flv = fl[sl_]; asm volatile("" ::: "memory"); LD_O(X, sl_); SBAR(); } while (0)
#define CK_O(X) do { if (__builtin_expect(__builtin_amdgcn_readfirstlane(flv) != RWANT(), 0)) { \
        const int sl_ = RSLOT(); \
        while (fl[sl_] != RWANT()) __builtin_amdgcn_s_sleep(1); \
        asm volatile("" ::: "memory"); LD_O(X, sl_); } SBAR(); } while (0)
#define RD_Q() do { const int jq_ = (2 * mt + t + 1) & 7; \
      qnl = *(const LAS bf16x8*)(L + oQL + jq_ * 1280); qnh = *(const LAS bf16x4*)(L + oQH + jq_ * sQH); SBAR(); } while (0)
#define ROUND(OC, OP) do { \
      STEP(SA, SB, 0, OP, OC, qcl, qch, ); \
      STEP(SB, SA, 1, OC, OC, qcl, qch, WR_O(OP, t)); \
      STEP(SA, SB, 2, OC, OC, qcl, qch, ); \
      STEP(SB, SA, 3, OC, OC, qcl, qch, RD_Q()); \
      STEP(SA, SB, 4, OC, OC, qcl, qch, ); \
      STEP(SB, SA, 5, OC, OC, qcl, qch, RD_O(OP)); \
      STEP(SA, SB, 6, OC, OC, qcl, qch, ); \
      STEP(SB, SA, 7, OC, OC, qnl, qnh, CK_O(OP)); \
      qcl = qnl; qch = qnh; \
    } while (0)
#pragma unroll 1
    for (int t = 0; t < 8; ++t) {
      ROUND(OA, OB);
      ++t;
      ROUND(OB, OA);
    }
    OB = MFMA16(vb[7], AS8(p1), OB);
    __builtin_amdgcn_s_setprio(0);
    if (kh == 0) {
      WR_O(OB, 8);
      if (pass == 0) {
        int ln = lane; PIN(ln);
        unsigned vw[5];
#pragma unroll
        for (int ph = 0; ph < 5; ++ph) { const int ci = ph * 64 + ln, rw = ci / 10; vw[ph] = rw * 1280 + (ci - rw * 10) * 16; }
#pragma unroll 1
        for (int gj = mt; gj < 10; gj += 4) {
          const char* src = (const char*)p.wo + (size_t)(gj < 5 ? gj * 32 : 320 + (gj - 5) * 32) * 1280 + hg * 160;
#pragma unroll
          for (int ph = 0; ph < 5; ++ph) glds(src, vw[ph], lds0 + L_WOA + gj * 5120 + ph * 1024);
        }
        if (hasG) {
#pragma unroll 1
          for (int f = mt; f < 20; f += 4)
            glds((const char*)(f < 10 ? p.wg : p.wu) + (size_t)(f < 10 ? f : f - 10) * (64 * FF * 2) + gsl * 16, ln * (FF * 2), lds0 + L_FGU + f * 1024);
        }
        if (hasD) {
#pragma unroll 1
          for (int d = mt; d < 27; d += 4) glds((const char*)p.wd + (size_t)d * (64 * 1280) + dsl * 16, ln * 1280, lds0 + L_FD + d * 1024);
        }
      }
      const int sl = 8 + ((2 * mt + 6) & 7);
      while (fl[sl] != (unsigned)(pass * 16 + 8)) __builtin_amdgcn_s_sleep(4);
      asm volatile("" ::: "memory");
      LD_O(OB, sl);
      LD_O(OA, sl - 8);
    }
    float F[12];
#pragma unroll
    for (int i = 0; i < 12; ++i) F[i] = OB[i] + OA[i];
    auto lq = __builtin_amdgcn_permlane32_swap(__float_as_uint(F[8]), __float_as_uint(F[8]), false, false);
    const float l = __uint_as_float(lq[1]);
    if (pass == 0 && __any(!(l >= 5.421010862427522e-20f))) fl[16] = 1u;
    {
      const int j = (2 * mt + 6 + kh) & 7, pos = (j & 1) * 32 + r;
      const float rl = __builtin_amdgcn_rcpf(l);
      char* ob = ws + WS_O + (size_t)rg * 81920 + (size_t)(pos >> 4) * 20480 + (pos & 15) * 16;
      const int col0 = hg * 80 + (j >> 1) * 20;
      auto put = [&](int d, float a0, float a1, float a2, float a3) {
        const int col = col0 + d, oc = col >> 3, gg = oc & 3, half = ((col >> 2) & 1) ^ (gg & 1);
        st_g64(ob + (oc >> 2) * 1024 + gg * 256 + half * 8, cvtpk(a0 * rl, a1 * rl), cvtpk(a2 * rl, a3 * rl));
      };
      put(4 * h, F[0], F[1], F[2], F[3]);
      put(8 + 4 * h, F[4], F[5], F[6], F[7]);
      if (h == 0) put(16, F[8], F[9], F[10], F[11]);
    }
    asm volatile("s_waitcnt vmcnt(0) lgkmcnt(0)\n\ts_barrier" ::: "memory");
    if (__builtin_expect(pass == 1 || fl[16] == 0u, 1)) break;
    {
      LAS float* mx = (LAS float*)(LW + L_OA);
#pragma unroll 1
      for (int j = 0; j < 8; ++j) {
        const bf16x8 ql = *(const LAS bf16x8*)(L + oQL + j * 1280);
        bf16x4 qh = *(const LAS bf16x4*)(L + L_Q + 1024 + r * 8 + j * 1280);
        if (h == 1) qh = (bf16x4){};
        float m = -INFINITY;
#pragma unroll
        for (int i = 0; i < 8; ++i) {
          const f32x16 sc = MFMA8(kx[i], qh, MFMA16(kl[i], ql, (f32x16){}));
#pragma unroll
          for (int e = 0; e < 16; ++e) m = fmaxf(m, sc[e]);
        }
        m = fmaxf(m, __shfl_xor(m, 32));
        if (h == 0) mx[(wid * 8 + j) * 32 + r] = m;
      }
      BAR_LGKM();
      float m = -INFINITY;
#pragma unroll
      for (int w = 0; w < 8; ++w) m = fmaxf(m, mx[(w * 8 + wid) * 32 + r]);
      const float nm = -m, a = bfr(nm), r1 = nm - a, bb = bfr(r1), cc = r1 - bb;
      if (h == 0) *(LAS u32x2*)(LW + L_QB + wid * 256 + r * 8) = (u32x2){cvtpk(a, bb), cvtpk(cc, 0.f)};
    }
  }
  if (tid < 8) st_g64(ws + WS_F2 + rg * 1024 + tid * 128 + hg * 8, 1u, 0u);
  int lane_t = lane; PIN(lane_t);
  {
  const int lane = lane_t, tid = wid * 64 + lane, c = lane & 15, g = lane >> 4, r = lane & 31;
  const int q4 = c >> 2, p4 = c & 3;
  const unsigned wA1 = (160 * kh + 8 * g + (g & 1) * 4 + q4) * 160 + p4 * 8, wA2 = (160 * kh + 8 * g + (1 - (g & 1)) * 4 + q4) * 160 + p4 * 8;
  const bool keep2 = (g >> 1) == kh;
  const lds_ptr exw = LW + mt * 5120 + lane * 16;
  {
    unsigned vw[5];
#pragma unroll
    for (int ph = 0; ph < 5; ++ph) { const int ci = ph * 64 + lane, rw = ci / 10; vw[ph] = rw * 1280 + (ci - rw * 10) * 16; }
    const char* src = (const char*)p.wo + (size_t)((wid & 1) * 320 + (wid >> 1) * 32 + 160) * 1280 + hg * 160;
#pragma unroll
    for (int ph = 0; ph < 5; ++ph) glds(src, vw[ph], lds0 + L_WOB + ((wid & 1) * 5 + (wid >> 1)) * 5120 + ph * 1024);
    if (wid < 2) {
      src = (const char*)p.wo + (size_t)(wid * 320 + 128 + 160) * 1280 + hg * 160;
#pragma unroll
      for (int ph = 0; ph < 5; ++ph) glds(src, vw[ph], lds0 + L_WOB + (wid * 5 + 4) * 5120 + ph * 1024);
    }
  }
  const int row = rg * 64 + mt * 16 + c;
  int colo[3];
  u32x2 xv[3], wv[3];
#pragma unroll
  for (int u = 0; u < 3; ++u) {
    colo[u] = (u == 2 ? 2 : u + 3 * kh) * 16 + 4 * g;
    xv[u] = *(const u32x2*)(p.x + (size_t)row * D + hg * 80 + colo[u]);
    wv[u] = *(const u32x2*)(p.wr + hg * 80 + colo[u]);
  }
  float sw = 0.f;
#pragma unroll
  for (int i = 0; i < 5; ++i) sw = dot2(((const unsigned*)p.wr)[lane + 64 * i], 0x3f803f80u, sw);
  for (unsigned spins = 0; spins < SPIN_CAP; ++spins) {
    const u64 f = ld_g64(ws + WS_F2 + rg * 1024 + hg * 128 + (kh * 4 + (lane & 3)) * 8);
    if (__all(f != INVALID)) break;
    __builtin_amdgcn_s_sleep(PSLEEP);
  }
  SBAR();
  u32x4 of[10];
  {
    const __amdgpu_buffer_rsrc_t rs = __builtin_amdgcn_make_buffer_rsrc((void*)(ws + WS_O + (size_t)rg * 81920), 0, 81920, 0x00020000);
#pragma unroll
    for (int s = 0; s < 10; ++s) of[s] = __builtin_amdgcn_raw_buffer_load_b128(rs, (mt * 20 + kh * 10) * 1024 + lane * 16 + s * 1024, 0, 16);
  }
  SBAR();
#pragma unroll
  for (int u = 0; u < 5; ++u) acc[u] = (f32x4){};
  PSTEPS(L_WOA, of, 0, false, false);
  BAR_LGKM();
  PSTEPS(L_WOB, of, 5, false, false);
  unsigned hw[3][2];
  {
    *(LAS f32x4*)(exw + L_EX + 3072 * (1 - kh)) = acc[3]; *(LAS f32x4*)(exw + L_EX + 3072 * (1 - kh) + 1024) = acc[4];
    if (!keep2) *(LAS f32x4*)(exw + L_EX + 2048) = acc[2];
    BAR_LGKM();
    float s1 = 0.f, dt = 0.f;
#pragma unroll
    for (int u = 0; u < 3; ++u) {
      const f32x4 a = acc[u] + *(const LAS f32x4*)(exw + L_EX + (u == 2 ? 2 : u + 3 * kh) * 1024);
      const unsigned a01 = cvtpk(a[0], a[1]), a23 = cvtpk(a[2], a[3]);
      hw[u][0] = cvtpk(bflo(xv[u][0]) + bflo(a01), bfhi(xv[u][0]) + bfhi(a01));
      hw[u][1] = cvtpk(bflo(xv[u][1]) + bflo(a23), bfhi(xv[u][1]) + bfhi(a23));
      const unsigned on = (u < 2 || keep2) ? 0x3f803f80u : 0u, w0 = (u < 2 || keep2) ? wv[u][0] : 0u, w1 = (u < 2 || keep2) ? wv[u][1] : 0u;
      if (u == 2 && !keep2) { hw[u][0] = 0u; hw[u][1] = 0u; }
      s1 = dot2(hw[u][0], on, dot2(hw[u][1], on, s1));
      dt = dot2(hw[u][0], w0, dot2(hw[u][1], w1, dt));
    }
    s1 = add32(add16(s1)); dt = add32(add16(dt));
    const float ml = s1 * (1.0f / 40.f);
    float m2 = 0.f;
#pragma unroll
    for (int u = 0; u < 3; ++u) {
      const float d0 = bflo(hw[u][0]) - ml, d1 = bfhi(hw[u][0]) - ml, d2 = bflo(hw[u][1]) - ml, d3 = bfhi(hw[u][1]) - ml;
      const float q = (d0 * d0 + d1 * d1) + (d2 * d2 + d3 * d3);
      m2 += (u < 2 || keep2) ? q : 0.f;
    }
    m2 = add32(add16(m2));
    LAS f32x4* st = (LAS f32x4*)(LW + L_ST2);
    if (g == 0) st[wid * 16 + c] = (f32x4){ml, m2, dt, 0.f};
    BAR_LGKM();
    if (wid == 0) {
      const f32x4 a = st[lane], bb = st[64 + lane];
      const float dm = a[0] - bb[0];
      const unsigned q0 = (__float_as_uint(0.5f * (a[0] + bb[0])) + 0x400u) >> 11, q1 = (__float_as_uint(a[1] + bb[1] + 20.f * dm * dm) + 0x400u) >> 11,
                     q2 = (__float_as_uint(a[2] + bb[2]) + 0x400u) >> 11;
      st_g64(ws + WS_ST + (size_t)(rg * 8 + hg) * 512 + lane * 8, q0 | (q1 << 21), (q1 >> 11) | (q2 << 10));
    }
  }
  {
    const char* sp = ws + WS_ST + (size_t)(rg * 8 + wid) * 512 + lane * 8;
    u64 a;
    for (unsigned spins = 0; spins < SPIN_CAP; ++spins) {
      a = ld_g64(sp);
      if (__all(a != INVALID)) break;
      __builtin_amdgcn_s_sleep(PSLEEP);
    }
    LAS float* gs = (LAS float*)(LW + L_GS) + wid * 192 + lane;
    gs[0] = __uint_as_float((unsigned)a << 11); gs[64] = __uint_as_float(((unsigned)(a >> 21)) << 11); gs[128] = __uint_as_float(((unsigned)(a >> 42)) << 11);
  }
  sw = add32(add16(addrow(sw)));
  BAR_LGKM();
  float mean_l, rstd_l, r_l; int best;
  {
    const LAS float* gs = (const LAS float*)(L + L_GS) + lane;
    float mu[8], mean = 0.f, M2 = 0.f, dt = 0.f;
#pragma unroll
    for (int i = 0; i < 8; ++i) { mu[i] = gs[i * 192]; mean += mu[i]; M2 += gs[i * 192 + 64]; dt += gs[i * 192 + 128]; }
    mean *= 0.125f;
#pragma unroll
    for (int i = 0; i < 8; ++i) { const float d = mu[i] - mean; M2 += 80.f * d * d; }
    mean_l = mean; rstd_l = __builtin_amdgcn_rsqf(M2 * (1.0f / D) + LN_EPS);
    r_l = rstd_l * (dt - mean * sw);
    const unsigned key = umax32(umax16(umaxrow((ordkey(r_l) & ~63u) | (unsigned)(63 - lane))));
    best = 63 - (int)(key & 63u);
  }
  if (hg == 0 && wid == 0) {
    const unsigned ck = (__builtin_amdgcn_readlane(ordkey(r_l), best) & ~2047u) | (unsigned)(2047 - (rg * 64 + best));
    if (lane < 32) st_g64(ws + WS_CAND + lane * 256 + rg * 8, ck, 0u);
  }
  const int lrow = mt * 16 + c;
  const float mean = __shfl(mean_l, lrow), rstd = __shfl(rstd_l, lrow);
  unsigned ow[3][2];
#pragma unroll
  for (int u = 0; u < 3; ++u) {
    const float h0 = bflo(hw[u][0]), h1 = bfhi(hw[u][0]), h2 = bflo(hw[u][1]), h3 = bfhi(hw[u][1]);
    const unsigned n01 = cvtpk((h0 - mean) * rstd, (h1 - mean) * rstd), n23 = cvtpk((h2 - mean) * rstd, (h3 - mean) * rstd);
    ow[u][0] = cvtpk(h0 + bflo(n01), h1 + bfhi(n01)); ow[u][1] = cvtpk(h2 + bflo(n23), h3 + bfhi(n23));
    if ((u < 2 || keep2) && lrow == best) {
      char* rw = ws + WS_ROW + rg * 2560 + (hg * 80 + colo[u]) * 2;
      st_val(rw, n01, n23); st_val(rw + 1280, hw[u][0], hw[u][1]);
    }
  }
  if (wid == 0) {
    u64 a;
    for (unsigned spins = 0; spins < SPIN_CAP; ++spins) {
      a = ld_g64(ws + WS_CAND + (b & 31) * 256 + r * 8);
      if (__all(a != INVALID)) break;
      __builtin_amdgcn_s_sleep(PSLEEP);
    }
    const unsigned key = umax16(umaxrow((unsigned)a));
    if (lane == 0) *(LAS unsigned*)(LW + L_RED + 1024) = key;
  }
  if (lrow != best) {
#pragma unroll
    for (int u = 0; u < 3; ++u)
      if (u < 2 || keep2) st_out((u32x2*)(p.out + (size_t)row * D + hg * 80 + colo[u]), (u32x2){ow[u][0], ow[u][1]});
  }
  BAR_LGKM();
  const unsigned win = *(const LAS unsigned*)(L + L_RED + 1024);
  const unsigned wk_ = (win & ~2047u) | 1024u;
  const float r_sel = __uint_as_float((wk_ & 0x80000000u) ? (wk_ & 0x7fffffffu) : ~wk_);
  const int widx = 2047 - (int)(win & 2047u), wrg = widx >> 6;
  if (wrg != rg && lrow == best) {
#pragma unroll
    for (int u = 0; u < 3; ++u)
      if (u < 2 || keep2) st_out((u32x2*)(p.out + (size_t)row * D + hg * 80 + colo[u]), (u32x2){ow[u][0], ow[u][1]});
  }
  if (hasG) {
    const char* rw = ws + WS_ROW + wrg * 2560;
    const int k0 = tid, k1 = tid < 128 ? tid + 512 : tid - 128, k2 = tid + 384 < 640 ? tid + 384 : 639;
    u32x4 fw[3];
#pragma unroll
    for (int j = 0; j < 3; ++j) { int e = tid + 512 * j; e = e < 1280 ? e : 1279; fw[j] = *(const LAS u32x4*)(L + L_FGU + e * 16); }
    u64 a0, a1, a2;
    for (unsigned spins = 0; spins < SPIN_CAP; ++spins) {
      a0 = ld_g64(rw + (k0 >> 2) * 8); a1 = ld_g64(rw + (k1 >> 2) * 8); a2 = ld_g64(rw + (k2 >> 2) * 8);
      if (__all((a0 != INVALID) & (a1 != INVALID) & (a2 != INVALID))) break;
      __builtin_amdgcn_s_sleep(PSLEEP);
    }
    const int sh = (tid & 3) * 16;
    const float x0 = bflo((unsigned)(a0 >> sh)), x1 = bflo((unsigned)(a1 >> sh)), x2 = tid < 256 ? bflo((unsigned)(a2 >> sh)) : 0.f;
    const float xg1 = tid < 128 ? x1 : 0.f, xu1 = tid < 128 ? 0.f : x1;
    float v8[8], v4[4];
#pragma unroll
    for (int j = 0; j < 4; ++j) {
      v8[2 * j] = rs32(x0 * bflo(fw[0][j]) + xg1 * bflo(fw[1][j]), xu1 * bflo(fw[1][j]) + x2 * bflo(fw[2][j]));
      v8[2 * j + 1] = rs32(x0 * bfhi(fw[0][j]) + xg1 * bfhi(fw[1][j]), xu1 * bfhi(fw[1][j]) + x2 * bfhi(fw[2][j]));
    }
#pragma unroll
    for (int j = 0; j < 4; ++j) v4[j] = addrow(rs16(v8[j], v8[4 + j]));
    LAS f32x4* red = (LAS f32x4*)(LW + L_RED);
    if (c == 0) red[wid * 4 + g] = (f32x4){v4[0], v4[1], v4[2], v4[3]};
    BAR_LGKM();
    if (wid == 0) {
      const LAS float* rf = (const LAS float*)(L + L_RED) + (lane & 7);
      float gv = 0.f, uv = 0.f;
#pragma unroll
      for (int w = 0; w < 8; ++w) { gv += rf[w * 16]; uv += rf[w * 16 + 8]; }
      gv = bfr(gv); uv = bfr(uv);
      const float act = bfr(gv * __builtin_amdgcn_rcpf(1.0f + __expf(-gv))) * uv;
      const unsigned pk = cvtpk(act, dppf<0xB1>(act));
      const unsigned pk2 = dppu<0x4E>(pk);
      if ((lane & 3) == 0) st_val(ws + WS_ACT + (lane >> 3) * 4096 + gsl * 16 + (lane & 4) * 2, pk, pk2);
    }
  }
  if (hasD) {
    const int tq = tid < 432 ? tid : 431;
    const int colb = dsl * 8;
    u32x4 fw[4];
#pragma unroll
    for (int j = 0; j < 4; ++j) fw[j] = *(const LAS u32x4*)(L + L_FD + (4 * tq + j) * 16);
    u64 a0, hn4, h4;
    if (!hasG) __builtin_amdgcn_s_sleep(ADELAY);
    for (unsigned spins = 0; spins < SPIN_CAP; ++spins) {
      a0 = ld_g64(ws + WS_ACT + xcd * 4096 + tq * 8);
      hn4 = ld_g64(ws + WS_ROW + wrg * 2560 + colb * 2 + (lane & 4) * 2); h4 = ld_g64(ws + WS_ROW + wrg * 2560 + 1280 + colb * 2 + (lane & 4) * 2);
      if (__all((a0 != INVALID) & (hn4 != INVALID) & (h4 != INVALID))) break;
      __builtin_amdgcn_s_sleep(ASLEEP);
    }
    float av[4] = {bflo((unsigned)a0), bfhi((unsigned)a0), bflo((unsigned)(a0 >> 32)), bfhi((unsigned)(a0 >> 32))};
    if (tid >= 432) { av[0] = av[1] = av[2] = av[3] = 0.f; }
    float y[8], v4[4], v2[2];
#pragma unroll
    for (int j = 0; j < 4; ++j) {
      y[2 * j] = (av[0] * bflo(fw[0][j]) + av[1] * bflo(fw[1][j])) + (av[2] * bflo(fw[2][j]) + av[3] * bflo(fw[3][j]));
      y[2 * j + 1] = (av[0] * bfhi(fw[0][j]) + av[1] * bfhi(fw[1][j])) + (av[2] * bfhi(fw[2][j]) + av[3] * bfhi(fw[3][j]));
    }
#pragma unroll
    for (int j = 0; j < 4; ++j) v4[j] = rs32(y[j], y[4 + j]);
#pragma unroll
    for (int j = 0; j < 2; ++j) v2[j] = addrow(rs16(v4[j], v4[2 + j]));
    LAS f32x2* red = (LAS f32x2*)(LW + L_RED + 512);
    if (c == 0) red[wid * 4 + g] = (f32x2){v2[0], v2[1]};
    BAR_LGKM();
    if (wid == 0 && lane < 8) {
      const LAS float* rf = (const LAS float*)(L + L_RED + 512) + lane;
      float tot = 0.f;
#pragma unroll
      for (int w = 0; w < 8; ++w) tot += rf[w * 8];
      const float rs = bfr(r_sel);
      const float sg = bfr(__builtin_amdgcn_rcpf(1.0f + __expf(-rs)));
      const float yv = bfr(bfr(tot) * sg);
      const float hv = bflo((unsigned)(h4 >> ((lane & 3) * 16))), hn = bflo((unsigned)(hn4 >> ((lane & 3) * 16)));
      p.out[(size_t)widx * D + colb + lane] = (u16)(cvtpk(hv + bfr(hn + yv), 0.f) & 0xffffu);
    }
  }
  }
}

extern "C" void kernel_launch(void* const* d_in, const int* in_sizes, int n_in, void* d_out, int out_size, void* d_ws, size_t ws_size, hipStream_t stream) {
  Params p;
  p.x = (const u16*)d_in[0]; p.wq = (const u16*)d_in[1]; p.wk = (const u16*)d_in[2]; p.wv = (const u16*)d_in[3]; p.wo = (const u16*)d_in[4];
  p.wr = (const u16*)d_in[6]; p.wg = (const u16*)d_in[7]; p.wu = (const u16*)d_in[8]; p.wd = (const u16*)d_in[9];
  p.out = (u16*)d_out; p.ws = (char*)d_ws;
  (void)hipFuncSetAttribute((const void*)fused_kernel, hipFuncAttributeMaxDynamicSharedMemorySize, LDS_BYTES);
  fused_kernel<<<256, 512, LDS_BYTES, stream>>>(p);
}
```

```cpp
#include <hip/hip_runtime.h>
#include <hip/hip_bf16.h>
#include <stdint.h>

typedef unsigned short u16;
typedef unsigned long long u64;
using bf16x8 = __attribute__((ext_vector_type(8))) short;
using bf16x4 = __attribute__((ext_vector_type(4))) short;
using f32x16 = __attribute__((ext_vector_type(16))) float;
using f32x4 = __attribute__((ext_vector_type(4))) float;
using f32x2 = __attribute__((ext_vector_type(2))) float;
using bfx2 = __attribute__((ext_vector_type(2))) __bf16;
using u32x4 = __attribute__((ext_vector_type(4))) unsigned;
using u32x2 = __attribute__((ext_vector_type(2))) unsigned;
#define LAS __attribute__((address_space(3)))
#define GAS __attribute__((address_space(1)))
typedef LAS const char* lds_cptr;
typedef LAS char* lds_ptr;
typedef GAS unsigned gu32;
typedef GAS u64 gu64;
#define RLX_AGENT __ATOMIC_RELAXED, __HIP_MEMORY_SCOPE_AGENT
#define SBAR() __builtin_amdgcn_sched_barrier(0)
#define PIN(x) asm volatile("" : "+v"(x))
#define MFMA16(a, b, c) __builtin_amdgcn_mfma_f32_32x32x16_bf16(a, b, c, 0, 0, 0)
#define MFMA8(a, b, c) __builtin_amdgcn_mfma_f32_32x32x8bf16_1k(a, b, c, 0, 0, 0)
#define MFMAS(a, b, c) __builtin_amdgcn_mfma_f32_16x16x32_bf16(a, b, c, 0, 0, 0)
#define DI __device__ __forceinline__
#define AS8(p) __builtin_bit_cast(bf16x8, p)
#define AS4(p) __builtin_bit_cast(bf16x4, p)

constexpr int S = 2048, D = 640, FF = 1728;
constexpr int TILE_B = 2624;
constexpr float QSCALE = 0.22360679774997896f * 1.4426950408889634f;
constexpr float LN_EPS = 1e-5f;
constexpr unsigned SPIN_CAP = 1u << 18;
#define PSLEEP 1
#define ADELAY 60
#define ASLEEP 2
#define OUTNT 0

constexpr size_t WS_EPOCH = 0;
constexpr size_t WS_F1 = 4096;
constexpr size_t WS_F2 = WS_F1 + 8 * 4096;
constexpr size_t WS_CAND = WS_F2 + 32 * 1024;
constexpr size_t WS_ACT = WS_CAND + 32 * 256;
constexpr size_t WS_ROW = WS_ACT + 8 * 4096;
constexpr size_t WS_ST = WS_ROW + 32 * 2560;
constexpr size_t WS_KV = WS_ST + 256 * 512;
constexpr size_t WS_O = WS_KV + 64 * TILE_B;
constexpr u64 INVALID = ~0ull;

constexpr int L_X = 0, L_WKV = 81920, L_WQA = L_WKV + 25664, L_SC = L_WQA + 51200;
constexpr int L_ST1 = 71680, L_CS = L_ST1 + 1024, L_MISC = L_CS + 640;
constexpr int L_WQB = 0, L_QX = 51200;
constexpr int L_WOA = 0;
constexpr int L_FGU = 51200;
constexpr int L_FD = L_FGU + 20480;
constexpr int L_OA = 99328;
constexpr int L_DUM = 148480;
constexpr int L_FL = 150528;
constexpr int L_Q = 151552;
constexpr int L_QB = L_Q + 8 * 1280;
constexpr int L_WOB = 99328;
constexpr int LDS_BYTES = 163840;
constexpr int L_EX = 0, L_ST2 = L_EX + 20480, L_GS = L_ST2 + 2048, L_RED = L_GS + 6144;

struct Params {
  const u16 *x, *wq, *wk, *wv, *wo, *wr, *wg, *wu, *wd;
  u16* out;
  char* ws;
};

DI unsigned cvtpk(float lo, float hi) { return __builtin_bit_cast(unsigned, __builtin_convertvector((f32x2){lo, hi}, bfx2)); }
DI float bflo(unsigned w) { return __uint_as_float(w << 16); }
DI float bfhi(unsigned w) { return __uint_as_float(w & 0xffff0000u); }
DI float bfr(float v) { return bflo(cvtpk(v, 0.f)); }
DI void glds(const void* sbase, unsigned voff, unsigned lds_dst) {
  unsigned sv;
  asm volatile("s_mov_b32 %0, m0\n\ts_mov_b32 m0, %3\n\ts_nop 0\n\tglobal_load_lds_dwordx4 %1, %2\n\ts_mov_b32 m0, %0" : "=&s"(sv) : "v"(voff), "s"(sbase), "s"(lds_dst) : "memory");
}
DI bf16x4 trread(lds_cptr p) { return AS4(__builtin_amdgcn_ds_read_tr16_b64_v4i16((LAS bf16x4*)p)); }
DI bf16x8 cat8(bf16x4 a, bf16x4 b) { return __builtin_shufflevector(a, b, 0, 1, 2, 3, 4, 5, 6, 7); }
DI float dot2(unsigned a, unsigned b, float c) { return __builtin_amdgcn_fdot2_f32_bf16(__builtin_bit_cast(bfx2, a), __builtin_bit_cast(bfx2, b), c, false); }
DI void st_g64(char* p, unsigned lo, unsigned hi) { __hip_atomic_store((gu64*)p, ((u64)hi << 32) | lo, RLX_AGENT); }
DI void st_out(u32x2* p, u32x2 v) { if (OUTNT == 1) __builtin_nontemporal_store(v, p); else if (OUTNT == 2) st_g64((char*)p, v[0], v[1]); else *p = v; }
DI void st_val(char* p, unsigned lo, unsigned hi) { st_g64(p, (lo & hi) == 0xffffffffu ? 0xfffffffeu : lo, hi); }
DI u64 ld_g64(const char* p) { return __hip_atomic_load((gu64*)p, RLX_AGENT); }
template <int CTRL> DI unsigned dppu(unsigned v) { return __builtin_amdgcn_update_dpp(0u, v, CTRL, 0xf, 0xf, true); }
template <int CTRL> DI float dppf(float v) { return __uint_as_float(dppu<CTRL>(__float_as_uint(v))); }
DI float add16(float v) { auto q = __builtin_amdgcn_permlane16_swap(__float_as_uint(v), __float_as_uint(v), false, false); return __uint_as_float(q[0]) + __uint_as_float(q[1]); }
DI float add32(float v) { auto q = __builtin_amdgcn_permlane32_swap(__float_as_uint(v), __float_as_uint(v), false, false); return __uint_as_float(q[0]) + __uint_as_float(q[1]); }
DI float addrow(float v) { v += dppf<0xB1>(v); v += dppf<0x4E>(v); v += dppf<0x141>(v); v += dppf<0x140>(v); return v; }
DI unsigned umax_(unsigned a, unsigned b) { return a > b ? a : b; }
DI unsigned umaxrow(unsigned v) { v = umax_(v, dppu<0xB1>(v)); v = umax_(v, dppu<0x4E>(v)); v = umax_(v, dppu<0x141>(v)); v = umax_(v, dppu<0x140>(v)); return v; }
DI unsigned umax16(unsigned v) { auto q = __builtin_amdgcn_permlane16_swap(v, v, false, false); return umax_(q[0], q[1]); }
DI unsigned umax32(unsigned v) { auto q = __builtin_amdgcn_permlane32_swap(v, v, false, false); return umax_(q[0], q[1]); }
DI float rs32(float a, float b) { auto q = __builtin_amdgcn_permlane32_swap(__float_as_uint(a), __float_as_uint(b), false, false); return __uint_as_float(q[0]) + __uint_as_float(q[1]); }
DI float rs16(float a, float b) { auto q = __builtin_amdgcn_permlane16_swap(__float_as_uint(a), __float_as_uint(b), false, false); return __uint_as_float(q[0]) + __uint_as_float(q[1]); }
DI unsigned ordkey(float f) { unsigned u = __float_as_uint(f); return (u & 0x80000000u) ? ~u : (u | 0x80000000u); }
#define BAR_LGKM() asm volatile("s_waitcnt lgkmcnt(0)\n\ts_barrier" ::: "memory")
#define DRAIN() asm volatile("s_waitcnt vmcnt(0)" ::: "memory")

#define STAMP(i)
#define DSTAMP(i)
#define STAMP_DECL
#define STAMP_OUT()

__global__ __launch_bounds__(512, 2) void fused_kernel(Params p) {
  extern __shared__ __attribute__((aligned(16))) char lds[];
  char* ws = p.ws;
  const int tid = threadIdx.x, lane = tid & 63;
  const int wid = __builtin_amdgcn_readfirstlane(tid >> 6);
  const int c = lane & 15, g = lane >> 4;
  const int r = lane & 31, h = lane >> 5;
  const int mt = wid & 3, kh = wid >> 2;
  const int b = blockIdx.x, xcd = b & 7, slot = b >> 3, rg = xcd * 4 + (slot >> 3), hg = slot & 7;
  const int kvmt = hg >> 1, isV = hg & 1;
  const bool hasG = slot < 27, hasD = slot >= 22;
  const int gsl = xcd * 27 + slot, dsl = xcd * 10 + slot - 22;
  const bool pub = (wid == 0);
  const bool team = (wid < 4);
  const int sw4 = mt;
  const unsigned lds0 = (unsigned)(uintptr_t)lds;
  const lds_cptr L = (lds_cptr)lds;
  const lds_ptr LW = (lds_ptr)lds;
  STAMP_DECL
  STAMP(0);
  const int q4 = c >> 2, p4 = c & 3;
  u32x4 xf[10];
  if (team) {
    const char* xs_ = (const char*)p.x + (size_t)(rg * 64 + kvmt * 16) * 1280 + wid * 320;
    const unsigned vo = c * 1280 + g * 16;
    asm volatile("global_load_dwordx4 %0, %5, %6\n\tglobal_load_dwordx4 %1, %5, %6 offset:64\n\tglobal_load_dwordx4 %2, %5, %6 offset:128\n\t"
                 "global_load_dwordx4 %3, %5, %6 offset:192\n\tglobal_load_dwordx4 %4, %5, %6 offset:256"
                 : "=&v"(xf[0]), "=&v"(xf[1]), "=&v"(xf[2]), "=&v"(xf[3]), "=&v"(xf[4]) : "v"(vo), "s"(xs_) : "memory");
    const char* src = (const char*)(isV ? p.wv : p.wk) + wid * 6400;
#pragma unroll
    for (int i = 0; i < 6; ++i) glds(src + i * 1024, lane * 16, lds0 + L_WKV + wid * 6400 + i * 1024);
    if (lane < 16) glds(src + 6144, lane * 16, lds0 + L_WKV + wid * 6400 + 6144);
  } else {
    const unsigned voff_x = g * 1280 + ((c ^ g ^ (4 * mt)) * 16);
#pragma unroll
    for (int m = 0; m < 4; ++m) {
      const char* src = (const char*)p.x + (size_t)(rg * 64 + 16 * m + 4 * mt) * 1280;
#pragma unroll
      for (int sg = 0; sg < 5; ++sg) glds(src + sg * 256, voff_x, lds0 + L_X + ((4 * m + mt) * 5 + sg) * 1024);
    }
  }
  unsigned voffw[5];
#pragma unroll
  for (int ph = 0; ph < 5; ++ph) { const int ci = ph * 64 + lane, rw = ci / 10; voffw[ph] = rw * 1280 + (ci - rw * 10) * 16; }
  auto dma_wgrp = [&](const u16* w, int row0, unsigned dst) {
    const char* src = (const char*)w + (size_t)row0 * 1280 + hg * 160;
#pragma unroll
    for (int ph = 0; ph < 5; ++ph) glds(src, voffw[ph], dst + ph * 1024);
  };
  SBAR();
  unsigned tag = 0;
  if (pub) tag = __hip_atomic_load((gu32*)(ws + WS_EPOCH), RLX_AGENT) + 1u;
  const int ro1 = 8 * g + (g & 1) * 4 + q4, ro2 = 8 * g + (1 - (g & 1)) * 4 + q4;
  bf16x8 ones = AS8(((u32x4){0x3f803f80u, 0x3f803f80u, 0x3f803f80u, 0x3f803f80u}));
  PIN(ones);
  auto diag = [&](const f32x4& gr) { return p4 == 0 ? gr[0] : p4 == 1 ? gr[1] : p4 == 2 ? gr[2] : gr[3]; };
  if (team) {
    if (wid == 1) {
      st_g64(ws + WS_ST + (size_t)(rg * 8 + hg) * 512 + lane * 8, ~0u, ~0u);
      char* a1 = lane < 40 ? ws + WS_ROW + rg * 2560 + (lane < 20 ? 0 : 1280 - 160) + hg * 160 + lane * 8
               : lane < 48 ? ws + WS_F2 + rg * 1024 + (lane - 40) * 128 + hg * 8 : ws + WS_ACT + ((lane - 48) >> 1) * 4096 + gsl * 16 + (lane & 1) * 8;
      if (lane < 48 || hasG) st_g64(a1, ~0u, ~0u);
      if (hg == 0 && lane < 32) st_g64(ws + WS_CAND + lane * 256 + rg * 8, ~0u, ~0u);
    }
    unsigned k1 = L_WKV + wid * 6400 + (8 * g + q4) * 40 + p4 * 8;
    unsigned pw = L_WKV + wid * 6400 + (isV ? (4 * g * 16 + c) * 4 : (c * 24 + 4 * g) * 4);
    PIN(k1); PIN(pw);
    asm volatile("s_waitcnt vmcnt(0)" : "+v"(xf[0]), "+v"(xf[1]), "+v"(xf[2]), "+v"(xf[3]), "+v"(xf[4]) :: "memory");
    f32x4 ax0 = {}, ax1 = {}, cx0 = {}, cx1 = {}, sm = {}, gr = {};
    bf16x8 wf[5][2];
#pragma unroll
    for (int s = 0; s < 5; ++s)
#pragma unroll
      for (int u = 0; u < 2; ++u) wf[s][u] = cat8(trread(L + k1 + s * 1280 + u * 32), trread(L + k1 + s * 1280 + u * 32 + 160));
#pragma unroll
    for (int s = 0; s < 5; ++s) { sm = MFMAS(ones, AS8(xf[s]), sm); gr = MFMAS(AS8(xf[s]), AS8(xf[s]), gr); }
#pragma unroll
    for (int s = 0; s < 5; ++s) {
      ax0 = MFMAS(wf[s][0], AS8(xf[s]), ax0); ax1 = MFMAS(wf[s][1], AS8(xf[s]), ax1);
      cx0 = MFMAS(wf[s][0], ones, cx0); cx1 = MFMAS(wf[s][1], ones, cx1);
    }
    SBAR();
    const lds_ptr pb = LW + L_WKV + wid * 6400;
    if (g == q4) *(LAS f32x2*)(pb + 1536 + c * 8) = (f32x2){sm[0], diag(gr)};
    if (c == 0) { *(LAS f32x4*)(pb + 1664 + g * 16) = cx0; *(LAS f32x4*)(pb + 1664 + 64 + g * 16) = cx1; }
    if (isV) {
#pragma unroll
      for (int i = 0; i < 4; ++i) *(LAS float*)(LW + pw + i * 64) = ax0[i];
      if (g == 0) {
#pragma unroll
        for (int i = 0; i < 4; ++i) *(LAS float*)(LW + pw + 1024 + i * 64) = ax1[i];
      }
    } else {
      *(LAS f32x4*)(LW + pw) = ax0;
      if (g == 0) *(LAS f32x4*)(LW + pw + 64) = ax1;
    }
  } else {
#pragma unroll
    for (int j = 0; j < 3; ++j) {
      const int idx = mt + 4 * j, gi = (idx & 1) * 5 + (idx >> 1);
      if (idx < 10) dma_wgrp(p.wq, (idx & 1) * 320 + (idx >> 1) * 32, lds0 + L_WQA + gi * 5120);
    }
    asm volatile("s_waitcnt vmcnt(10)" ::: "memory");
  }
  unsigned xa[4];
#pragma unroll
  for (int u = 0; u < 4; ++u) { xa[u] = L_X + (4 * mt + q4) * 5120 + p4 * 256 + ((g ^ p4) * 16) + (((4 * ((u + 2 * kh) & 3)) ^ (c & 12)) * 16) + (g & 1) * 8 + ((u + 10 * kh) >> 2) * 1024; PIN(xa[u]); }
  BAR_LGKM();
  STAMP(1);
#pragma unroll
  for (int s = 0; s < 10; ++s) {
    const unsigned a_ = xa[s & 3] + ((s >> 2) * 1024);
    const u32x2 a = *(const LAS u32x2*)(L + a_), bb = *(const LAS u32x2*)(L + (a_ ^ 8));
    xf[s] = (u32x4){a[0], a[1], bb[0], bb[1]};
  }
  BAR_LGKM();
  STAMP(2);
  float kn2 = 0.f;
  if (pub) {
    const lds_cptr pb = L + L_WKV;
    const int gm = rg * 4 + kvmt;
    char* tb = ws + WS_KV + (size_t)(gm >> 1) * TILE_B;
    auto kv_stats = [&](int pp, float& mean, float& rstd) {
      const f32x2 a = (*(const LAS f32x2*)(pb + 1536 + pp * 8) + *(const LAS f32x2*)(pb + 6400 + 1536 + pp * 8)) +
                      (*(const LAS f32x2*)(pb + 12800 + 1536 + pp * 8) + *(const LAS f32x2*)(pb + 19200 + 1536 + pp * 8));
      mean = a[0] * (1.0f / D);
      rstd = __builtin_amdgcn_rsqf(fmaxf(a[1] * (1.0f / D) - mean * mean, 0.f) + LN_EPS);
    };
    if (!isV) {
      float mean, rstd; kv_stats(c, mean, rstd);
      f32x4 a0 = {}, a1 = {}, c0 = {}, c1 = {};
#pragma unroll
      for (int w = 0; w < 4; ++w) {
        a0 += *(const LAS f32x4*)(pb + w * 6400 + (c * 24 + 4 * g) * 4); a1 += *(const LAS f32x4*)(pb + w * 6400 + (c * 24 + 16) * 4);
        c0 += *(const LAS f32x4*)(pb + w * 6400 + 1664 + g * 16); c1 += *(const LAS f32x4*)(pb + w * 6400 + 1664 + 64);
      }
      const unsigned w0 = cvtpk((a0[0] - mean * c0[0]) * rstd, (a0[1] - mean * c0[1]) * rstd), w1 = cvtpk((a0[2] - mean * c0[2]) * rstd, (a0[3] - mean * c0[3]) * rstd);
      const unsigned w2 = cvtpk((a1[0] - mean * c1[0]) * rstd, (a1[1] - mean * c1[1]) * rstd), w3 = cvtpk((a1[2] - mean * c1[2]) * rstd, (a1[3] - mean * c1[3]) * rstd);
      const int rr = (gm & 1) * 16 + c;
      st_g64(tb + ((g >> 1) * 32 + rr) * 16 + (g & 1) * 8, w0, w1);
      if (g == 0) st_g64(tb + 1024 + rr * 8, w2, w3);
      const float n2 = add32(add16(dot2(w0, w0, dot2(w1, w1, 0.f)))) + dot2(w2, w2, dot2(w3, w3, 0.f));
      kn2 = __uint_as_float(umaxrow(__float_as_uint(n2)));
    } else {
      f32x4 a0 = {}, a1 = {}; float cs0 = 0.f, cs1 = 0.f;
#pragma unroll
      for (int w = 0; w < 4; ++w) {
        a0 += *(const LAS f32x4*)(pb + w * 6400 + (c * 16 + 4 * g) * 4); a1 += *(const LAS f32x4*)(pb + w * 6400 + ((16 + p4) * 16 + 4 * g) * 4);
        cs0 += *(const LAS float*)(pb + w * 6400 + 1664 + c * 4); cs1 += *(const LAS float*)(pb + w * 6400 + 1664 + 64 + p4 * 4);
      }
      float v0[4], v1[4];
#pragma unroll
      for (int i = 0; i < 4; ++i) {
        float mean, rstd; kv_stats(4 * g + i, mean, rstd);
        v0[i] = (a0[i] - mean * cs0) * rstd; v1[i] = (a1[i] - mean * cs1) * rstd;
      }
      char* vb = tb + 1280 + (((gm & 1) * 2 + (g & 1)) * 21) * 16 + (g >> 1) * 8;
      st_g64(vb + c * 16, cvtpk(v0[0], v0[1]), cvtpk(v0[2], v0[3]));
      if (c < 5) st_g64(vb + (16 + c) * 16, c < 4 ? cvtpk(v1[0], v1[1]) : 0x3f803f80u, c < 4 ? cvtpk(v1[2], v1[3]) : 0x3f803f80u);
    }
  } else {
    const int w7 = wid - 1;
    dma_wgrp(p.wq, (w7 & 1) * 320 + (w7 >> 1) * 32 + 160, lds0 + L_WQB + ((w7 & 1) * 5 + (w7 >> 1)) * 5120);
    if (w7 < 3) { const int idx = w7 + 7; dma_wgrp(p.wq, (idx & 1) * 320 + (idx >> 1) * 32 + 160, lds0 + L_WQB + ((idx & 1) * 5 + (idx >> 1)) * 5120); }
  }
  {
    f32x4 sm = {}, gr = {};
#pragma unroll
    for (int s = 0; s < 10; ++s) { sm = MFMAS(ones, AS8(xf[s]), sm); gr = MFMAS(AS8(xf[s]), AS8(xf[s]), gr); }
    if (g == q4) *(LAS f32x2*)(LW + L_ST1 + (wid * 16 + c) * 8) = (f32x2){sm[0], diag(gr)};
  }
  SBAR();
  asm volatile("s_waitcnt vmcnt(5)\n\ts_barrier" ::: "memory");
  if (pub) {
    DRAIN();
    if (lane < 16) st_g64(ws + WS_F1 + (lane >> 1) * 4096 + b * 16 + (lane & 1) * 8, (lane & 1) == 0 ? __float_as_uint(kn2) : ~tag ^ 0x5a5a5a5au, tag);
  }
  STAMP(3);
  const unsigned wA1 = (160 * kh + ro1) * 160 + p4 * 8, wA2 = (160 * kh + ro2) * 160 + p4 * 8;
  f32x4 acc[5] = {}, csa = {}, csb = {};
  bf16x8 kl[8], va[8], vb[8]; bf16x4 kx[8];
  const __amdgpu_buffer_rsrc_t rskv = __builtin_amdgcn_make_buffer_rsrc((void*)(ws + WS_KV), 0, 64 * TILE_B, 0x00020000);
  const unsigned kvb = wid * 8 * TILE_B;
  const unsigned oKA = kvb + lane * 16, oKB = kvb + (h == 0 ? 1024 + r * 8 : 1280 + 20 * 16);
  const unsigned oV = r <= 20 ? kvb + 1280 + (h * 21 + r) * 16 : 0x40000000u;
#define LOAD_KV(i) asm volatile("buffer_load_dwordx4 %0, %4, %7, %8 offen sc1\n\tbuffer_load_dwordx2 %1, %5, %7, %8 offen sc1\n\t" \
      "buffer_load_dwordx4 %2, %6, %7, %8 offen sc1\n\tbuffer_load_dwordx4 %3, %6, %7, %8 offen offset:672 sc1" \
      : "+v"(kl[i]), "+v"(kx[i]), "+v"(va[i]), "+v"(vb[i]) : "v"(oKA), "v"(oKB), "v"(oV), "s"(rskv), "s"((i) * TILE_B) : "memory")
#define LOAD_KV_ALL() do { LOAD_KV(0); LOAD_KV(1); LOAD_KV(2); LOAD_KV(3); LOAD_KV(4); LOAD_KV(5); LOAD_KV(6); LOAD_KV(7); } while (0)
#define RDW(dst, o1, o2, off) dst = cat8(trread(L + (o1) + (off)), trread(L + (o2) + (off)))
#define RDU(buf, s, u) do { \
      if ((u) == 0) RDW(wf[buf][0], a1, a2, (s) * 5120); if ((u) == 1) RDW(wf[buf][1], a1, a2, (s) * 5120 + 32); if ((u) == 2) RDW(wf[buf][2], b1, b2, (s) * 5120); \
      if ((u) == 3) RDW(wf[buf][3], c1, c2, (s) * 5120); if ((u) == 4) RDW(wf[buf][4], c1, c2, (s) * 5120 + 32); \
      if ((u) == 5) RDW(wf[buf][5], d1, d2, (s) * 5120); if ((u) == 6) RDW(wf[buf][6], b1, b2, (s) * 5120 + 64); } while (0)
#define PSTEPS(base, XF, s0, CS, KV) do { \
    const unsigned a1 = (base) + wA1 + 96 * kh, a2 = (base) + wA2 + 96 * kh, b1 = (base) + wA1 + 64, b2 = (base) + wA2 + 64; \
    const unsigned c1 = (base) + wA1 + 96 - 96 * kh, c2 = (base) + wA2 + 96 - 96 * kh, d1 = (base) + wA1 + 32 * mt, d2 = (base) + wA2 + 32 * mt; \
    bf16x8 wf[2][7]; \
    _Pragma("unroll") for (int u = 0; u < ((CS) ? 7 : 5); ++u) RDU(0, 0, u); \
    SBAR(); \
    _Pragma("unroll") for (int s = 0; s < 5; ++s) { \
      const bf16x8 xs = AS8(XF[(s0) + s]); \
      _Pragma("unroll") for (int u = 0; u < ((CS) ? 7 : 5); ++u) { \
        if (u < 5) { acc[u] = MFMAS(wf[s & 1][u], xs, acc[u]); PIN(acc[u]); } \
        else if (u == 5) { csa = MFMAS(wf[s & 1][5], ones, csa); PIN(csa); } \
        else { csb = MFMAS(wf[s & 1][6], ones, csb); PIN(csb); } \
        SBAR(); \
        if (s + 1 < 5) RDU((s + 1) & 1, s + 1, u); \
        if ((KV) && s < 4 && u == 0 && kh == 0) { LOAD_KV(2 * (s & 3)); LOAD_KV(2 * (s & 3) + 1); }     \
        SBAR(); \
      } \
    } } while (0)
  PSTEPS(L_WQA, xf, 0, true, false);
  STAMP(17);
  if (pub) {
    unsigned km = 0;
    for (unsigned spins = 0; spins < SPIN_CAP; ++spins) {
      bool ok = true; km = 0;
#pragma unroll
      for (int i = 0; i < 4; ++i) {
        const u64 a = ld_g64(ws + WS_F1 + xcd * 4096 + (i * 64 + lane) * 16), bb = ld_g64(ws + WS_F1 + xcd * 4096 + (i * 64 + lane) * 16 + 8);
        ok &= ((unsigned)(a >> 32) == tag) & ((unsigned)(bb >> 32) == tag) & ((unsigned)bb == (~tag ^ 0x5a5a5a5au));
        km = umax_(km, (unsigned)a);
      }
      if (__all(ok)) break;
      __builtin_amdgcn_s_sleep(1);
    }
    km = umax32(umax16(umaxrow(km)));
    if (lane == 0) *(LAS unsigned*)(LW + L_MISC) = km;
    if (b == 0 && lane == 0) __hip_atomic_store((gu32*)(ws + WS_EPOCH), tag, RLX_AGENT);
  } else {
    DRAIN();
  }
  BAR_LGKM();
  STAMP(4);
  if (tid < 17) *(LAS unsigned*)(LW + L_FL + tid * 4) = 0u;
#pragma unroll
  for (int i = 0; i < 8; ++i) asm volatile("" : "=v"(kl[i]), "=v"(kx[i]), "=v"(va[i]), "=v"(vb[i]));
  PSTEPS(L_WQB, xf, 5, true, true);
  STAMP(15);
  const bool keep2 = (g >> 1) == kh;
  const lds_ptr exw = LW + mt * 5120 + lane * 16;
  {
    LAS float* csp = (LAS float*)(LW + L_CS);
    if (c == 0) {
      *(LAS f32x4*)(csp + (kh * 5 + mt) * 16 + 4 * g) = csa;
      if (mt == 0) *(LAS f32x4*)(csp + (kh * 5 + 4) * 16 + 4 * g) = csb;
    }
    *(LAS f32x4*)(exw + L_QX + 3072 * (1 - kh)) = acc[3]; *(LAS f32x4*)(exw + L_QX + 3072 * (1 - kh) + 1024) = acc[4];
    if (!keep2) *(LAS f32x4*)(exw + L_QX + 2048) = acc[2];
    const float km2 = *(const LAS float*)(L + L_MISC);
    const f32x2 sa = *(const LAS f32x2*)(L + L_ST1 + (mt * 16 + c) * 8), sb = *(const LAS f32x2*)(L + L_ST1 + ((mt + 4) * 16 + c) * 8);
    const float mean = (sa[0] + sb[0]) * (1.0f / D);
    const float rstd = __builtin_amdgcn_rsqf(fmaxf((sa[1] + sb[1]) * (1.0f / D) - mean * mean, 0.f) + LN_EPS) * QSCALE;
    BAR_LGKM();
    const int ph = mt >> 1, rr = (mt & 1) * 16 + c;
#pragma unroll
    for (int u = 0; u < 3; ++u) {
      const int t = u == 2 ? 2 : u + 3 * kh;
      const f32x4 a = acc[u] + *(const LAS f32x4*)(exw + L_QX + t * 1024);
      const f32x4 cv = *(const LAS f32x4*)(csp + t * 16 + 4 * g) + *(const LAS f32x4*)(csp + (5 + t) * 16 + 4 * g);
      const int n = 16 * t + 4 * g, hh = (n * 205) >> 12, d = n - hh * 20;
      const unsigned dst = L_Q + (hh * 2 + ph) * 1280 + (d < 16 ? ((d >> 3) * 32 + rr) * 16 + (d & 4) * 2 : 1024 + rr * 8);
      const u32x2 qv = (u32x2){cvtpk((a[0] - mean * cv[0]) * rstd, (a[1] - mean * cv[1]) * rstd), cvtpk((a[2] - mean * cv[2]) * rstd, (a[3] - mean * cv[3]) * rstd)};
      if (u < 2 || keep2) *(LAS u32x2*)(LW + dst) = qv;
    }
    BAR_LGKM();
    const u32x4 ql = *(const LAS u32x4*)(L + L_Q + wid * 1280 + lane * 16);
    const u32x2 qh = *(const LAS u32x2*)(L + L_Q + wid * 1280 + 1024 + r * 8);
    float n2 = dot2(ql[0], ql[0], dot2(ql[1], ql[1], 0.f)) + dot2(ql[2], ql[2], dot2(ql[3], ql[3], 0.f));
    if (h == 0) n2 += dot2(qh[0], qh[0], dot2(qh[1], qh[1], 0.f));
    n2 = add32(n2);
    const float M = __builtin_amdgcn_sqrtf(n2 * km2) * 1.0078125f;
    if (h == 0) *(LAS u32x2*)(LW + L_QB + wid * 256 + r * 8) = (u32x2){cvtpk(-M, 0.f), 0u};
  }
  STAMP(16);
  STAMP(5);
  const unsigned oQL = L_Q + lane * 16, oQH = h == 0 ? L_Q + 1024 + r * 8 : L_QB + r * 8;
  const unsigned sQH = h == 0 ? 1280 : 256;
  volatile LAS unsigned* fl = (volatile LAS unsigned*)(LW + L_FL);
#pragma unroll 1
  for (int pass = 0; pass < 2; ++pass) {
    BAR_LGKM();
    if (pass == 0 && kh == 1) LOAD_KV_ALL();
    f32x16 OA = {}, OB = {}, SA, SB;
    u32x4 p0, p1 = {0, 0, 0, 0};
    bf16x8 qcl, qnl; bf16x4 qch, qnh;
    unsigned flv;
    qcl = *(const LAS bf16x8*)(L + oQL + 2 * mt * 1280);
    qch = *(const LAS bf16x4*)(L + oQH + 2 * mt * sQH);
    DRAIN();
    if (kh == 0) __builtin_amdgcn_s_setprio(2); else __builtin_amdgcn_s_setprio(1);
    SBAR();
    DSTAMP(23);
    SA = MFMA8(kx[0], qch, MFMA16(kl[0], qcl, (f32x16){}));
    SBAR();
#define EXG(X, i, P, j) do { X[i] = __builtin_amdgcn_exp2f(X[i]); X[i + 1] = __builtin_amdgcn_exp2f(X[i + 1]); P[j] = cvtpk(X[i], X[i + 1]); \
                             X[i + 2] = __builtin_amdgcn_exp2f(X[i + 2]); X[i + 3] = __builtin_amdgcn_exp2f(X[i + 3]); P[j + 1] = cvtpk(X[i + 2], X[i + 3]); PIN(P); SBAR(); } while (0)
#define STEP(C, N, i, OP, OC, QL, QH, EXTRA) do { \
      N = MFMA16(kl[((i) + 1) & 7], QL, (f32x16){}); PIN(N); SBAR(); \
      EXG(C, 0, p0, 0); \
      OP = MFMA16(vb[((i) + 7) & 7], AS8(p1), OP); PIN(OP); SBAR(); \
      EXG(C, 4, p0, 2); \
      N = MFMA8(kx[((i) + 1) & 7], QH, N); PIN(N); SBAR(); \
      EXTRA; \
      EXG(C, 8, p1, 0); \
      OC = MFMA16(va[i], AS8(p0), OC); PIN(OC); SBAR(); \
      EXG(C, 12, p1, 2); \
    } while (0)
#define WR_O(X, tt) do { const int sl_ = kh * 8 + ((2 * mt + (tt) + 7) & 7); lds_ptr d_ = LW + L_OA + sl_ * 3072 + lane * 16; \
      *(LAS f32x4*)(d_) = (f32x4){X[0], X[1], X[2], X[3]}; *(LAS f32x4*)(d_ + 1024) = (f32x4){X[4], X[5], X[6], X[7]}; \
      *(LAS f32x4*)(d_ + 2048) = (f32x4){X[8], X[9], X[10], X[11]}; asm volatile("" ::: "memory"); \
      fl[sl_] = pass * 16 + (tt) + 1; PIN(X); SBAR(); } while (0)
#define LD_O(X, sl_) do { lds_cptr s_ = L + L_OA + (sl_) * 3072 + lane * 16; \
      const f32x4 a_ = *(const LAS f32x4*)(s_), b_ = *(const LAS f32x4*)(s_ + 1024), c_ = *(const LAS f32x4*)(s_ + 2048); \
      X[0] = a_[0]; X[1] = a_[1]; X[2] = a_[2]; X[3] = a_[3]; X[4] = b_[0]; X[5] = b_[1]; X[6] = b_[2]; X[7] = b_[3]; \
      X[8] = c_[0]; X[9] = c_[1]; X[10] = c_[2]; X[11] = c_[3]; } while (0)
#define RSLOT() (t < 7 ? kh * 8 + ((2 * mt + t + 1) & 7) : ((2 * mt + 6 + kh) & 7))
#define RWANT() ((unsigned)(pass * 16 + t + 1 + ((t == 7) ? kh : 0)))
#define RD_O(X) do { const int sl_ = RSLOT(); flv = fl[sl_]; asm volatile("" ::: "memory"); LD_O(X, sl_); SBAR(); } while (0)
#define CK_O(X) do { if (__builtin_expect(__builtin_amdgcn_readfirstlane(flv) != RWANT(), 0)) { \
        const int sl_ = RSLOT(); \
        while (fl[sl_] != RWANT()) __builtin_amdgcn_s_sleep(1); \
        asm volatile("" ::: "memory"); LD_O(X, sl_); } SBAR(); } while (0)
#define RD_Q() do { const int jq_ = (2 * mt + t + 1) & 7; \
      qnl = *(const LAS bf16x8*)(L + oQL + jq_ * 1280); qnh = *(const LAS bf16x4*)(L + oQH + jq_ * sQH); SBAR(); } while (0)
#define ROUND(OC, OP) do { \
      STEP(SA, SB, 0, OP, OC, qcl, qch, ); \
      STEP(SB, SA, 1, OC, OC, qcl, qch, WR_O(OP, t)); \
      STEP(SA, SB, 2, OC, OC, qcl, qch, ); \
      STEP(SB, SA, 3, OC, OC, qcl, qch, RD_Q()); \
      STEP(SA, SB, 4, OC, OC, qcl, qch, ); \
      STEP(SB, SA, 5, OC, OC, qcl, qch, RD_O(OP)); \
      STEP(SA, SB, 6, OC, OC, qcl, qch, ); \
      STEP(SB, SA, 7, OC, OC, qnl, qnh, CK_O(OP)); \
      qcl = qnl; qch = qnh; \
    } while (0)
#pragma unroll 1
    for (int t = 0; t < 8; ++t) {
      ROUND(OA, OB);
      ++t;
      ROUND(OB, OA);
    }
    OB = MFMA16(vb[7], AS8(p1), OB);
    __builtin_amdgcn_s_setprio(0);
    DSTAMP(14);
    if (kh == 0) {
      WR_O(OB, 8);
      if (pass == 0) {
        int ln = lane; PIN(ln);
        unsigned vw[5];
#pragma unroll
        for (int ph = 0; ph < 5; ++ph) { const int ci = ph * 64 + ln, rw = ci / 10; vw[ph] = rw * 1280 + (ci - rw * 10) * 16; }
#pragma unroll 1
        for (int gj = mt; gj < 10; gj += 4) {
          const char* src = (const char*)p.wo + (size_t)(gj < 5 ? gj * 32 : 320 + (gj - 5) * 32) * 1280 + hg * 160;
#pragma unroll
          for (int ph = 0; ph < 5; ++ph) glds(src, vw[ph], lds0 + L_WOA + gj * 5120 + ph * 1024);
        }
        DSTAMP(20);
        if (hasG) {
#pragma unroll 1
          for (int f = mt; f < 20; f += 4)
            glds((const char*)(f < 10 ? p.wg : p.wu) + (size_t)(f < 10 ? f : f - 10) * (64 * FF * 2) + gsl * 16, ln * (FF * 2), lds0 + L_FGU + f * 1024);
        }
        DSTAMP(21);
        if (hasD) {
#pragma unroll 1
          for (int d = mt; d < 27; d += 4) glds((const char*)p.wd + (size_t)d * (64 * 1280) + dsl * 16, ln * 1280, lds0 + L_FD + d * 1024);
        }
        DSTAMP(22);
      }
      const int sl = 8 + ((2 * mt + 6) & 7);
      while (fl[sl] != (unsigned)(pass * 16 + 8)) __builtin_amdgcn_s_sleep(4);
      asm volatile("" ::: "memory");
      LD_O(OB, sl);
      LD_O(OA, sl - 8);
    }
    STAMP(6);
    float F[12];
#pragma unroll
    for (int i = 0; i < 12; ++i) F[i] = OB[i] + OA[i];
    auto lq = __builtin_amdgcn_permlane32_swap(__float_as_uint(F[8]), __float_as_uint(F[8]), false, false);
    const float l = __uint_as_float(lq[1]);
    if (pass == 0 && __any(!(l >= 5.421010862427522e-20f))) fl[16] = 1u;
    {
      const int j = (2 * mt + 6 + kh) & 7, pos = (j & 1) * 32 + r;
      const float rl = __builtin_amdgcn_rcpf(l);
      char* ob = ws + WS_O + (size_t)rg * 81920 + (size_t)(pos >> 4) * 20480 + (pos & 15) * 16;
      const int col0 = hg * 80 + (j >> 1) * 20;
      auto put = [&](int d, float a0, float a1, float a2, float a3) {
        const int col = col0 + d, oc = col >> 3, gg = oc & 3, half = ((col >> 2) & 1) ^ (gg & 1);
        st_g64(ob + (oc >> 2) * 1024 + gg * 256 + half * 8, cvtpk(a0 * rl, a1 * rl), cvtpk(a2 * rl, a3 * rl));
      };
      put(4 * h, F[0], F[1], F[2], F[3]);
      put(8 + 4 * h, F[4], F[5], F[6], F[7]);
      if (h == 0) put(16, F[8], F[9], F[10], F[11]);
    }
    asm volatile("s_waitcnt vmcnt(0) lgkmcnt(0)\n\ts_barrier" ::: "memory");
    if (__builtin_expect(pass == 1 || fl[16] == 0u, 1)) break;
    {
      LAS float* mx = (LAS float*)(LW + L_OA);
#pragma unroll 1
      for (int j = 0; j < 8; ++j) {
        const bf16x8 ql = *(const LAS bf16x8*)(L + oQL + j * 1280);
        bf16x4 qh = *(const LAS bf16x4*)(L + L_Q + 1024 + r * 8 + j * 1280);
        if (h == 1) qh = (bf16x4){};
        float m = -INFINITY;
#pragma unroll
        for (int i = 0; i < 8; ++i) {
          const f32x16 sc = MFMA8(kx[i], qh, MFMA16(kl[i], ql, (f32x16){}));
#pragma unroll
          for (int e = 0; e < 16; ++e) m = fmaxf(m, sc[e]);
        }
        m = fmaxf(m, __shfl_xor(m, 32));
        if (h == 0) mx[(wid * 8 + j) * 32 + r] = m;
      }
      BAR_LGKM();
      float m = -INFINITY;
#pragma unroll
      for (int w = 0; w < 8; ++w) m = fmaxf(m, mx[(w * 8 + wid) * 32 + r]);
      const float nm = -m, a = bfr(nm), r1 = nm - a, bb = bfr(r1), cc = r1 - bb;
      if (h == 0) *(LAS u32x2*)(LW + L_QB + wid * 256 + r * 8) = (u32x2){cvtpk(a, bb), cvtpk(cc, 0.f)};
    }
  }
  if (tid < 8) st_g64(ws + WS_F2 + rg * 1024 + tid * 128 + hg * 8, 1u, 0u);
  int lane_t = lane; PIN(lane_t);
  {
  const int lane = lane_t, tid = wid * 64 + lane, c = lane & 15, g = lane >> 4, r = lane & 31;
  const int q4 = c >> 2, p4 = c & 3;
  const unsigned wA1 = (160 * kh + 8 * g + (g & 1) * 4 + q4) * 160 + p4 * 8, wA2 = (160 * kh + 8 * g + (1 - (g & 1)) * 4 + q4) * 160 + p4 * 8;
  const bool keep2 = (g >> 1) == kh;
  const lds_ptr exw = LW + mt * 5120 + lane * 16;
  {
    unsigned vw[5];
#pragma unroll
    for (int ph = 0; ph < 5; ++ph) { const int ci = ph * 64 + lane, rw = ci / 10; vw[ph] = rw * 1280 + (ci - rw * 10) * 16; }
    const char* src = (const char*)p.wo + (size_t)((wid & 1) * 320 + (wid >> 1) * 32 + 160) * 1280 + hg * 160;
#pragma unroll
    for (int ph = 0; ph < 5; ++ph) glds(src, vw[ph], lds0 + L_WOB + ((wid & 1) * 5 + (wid >> 1)) * 5120 + ph * 1024);
    if (wid < 2) {
      src = (const char*)p.wo + (size_t)(wid * 320 + 128 + 160) * 1280 + hg * 160;
#pragma unroll
      for (int ph = 0; ph < 5; ++ph) glds(src, vw[ph], lds0 + L_WOB + (wid * 5 + 4) * 5120 + ph * 1024);
    }
  }
  STAMP(7);
  const int row = rg * 64 + mt * 16 + c;
  int colo[3];
  u32x2 xv[3], wv[3];
#pragma unroll
  for (int u = 0; u < 3; ++u) {
    colo[u] = (u == 2 ? 2 : u + 3 * kh) * 16 + 4 * g;
    xv[u] = *(const u32x2*)(p.x + (size_t)row * D + hg * 80 + colo[u]);
    wv[u] = *(const u32x2*)(p.wr + hg * 80 + colo[u]);
  }
  float sw = 0.f;
#pragma unroll
  for (int i = 0; i < 5; ++i) sw = dot2(((const unsigned*)p.wr)[lane + 64 * i], 0x3f803f80u, sw);
  for (unsigned spins = 0; spins < SPIN_CAP; ++spins) {
    const u64 f = ld_g64(ws + WS_F2 + rg * 1024 + hg * 128 + (kh * 4 + (lane & 3)) * 8);
    if (__all(f != INVALID)) break;
    __builtin_amdgcn_s_sleep(PSLEEP);
  }
  SBAR();
  STAMP(18);
  u32x4 of[10];
  {
    const __amdgpu_buffer_rsrc_t rs = __builtin_amdgcn_make_buffer_rsrc((void*)(ws + WS_O + (size_t)rg * 81920), 0, 81920, 0x00020000);
#pragma unroll
    for (int s = 0; s < 10; ++s) of[s] = __builtin_amdgcn_raw_buffer_load_b128(rs, (mt * 20 + kh * 10) * 1024 + lane * 16 + s * 1024, 0, 16);
  }
  SBAR();
  STAMP(8);
#pragma unroll
  for (int u = 0; u < 5; ++u) acc[u] = (f32x4){};
  PSTEPS(L_WOA, of, 0, false, false);
  BAR_LGKM();
  PSTEPS(L_WOB, of, 5, false, false);
  STAMP(19);
  unsigned hw[3][2];
  {
    *(LAS f32x4*)(exw + L_EX + 3072 * (1 - kh)) = acc[3]; *(LAS f32x4*)(exw + L_EX + 3072 * (1 - kh) + 1024) = acc[4];
    if (!keep2) *(LAS f32x4*)(exw + L_EX + 2048) = acc[2];
    BAR_LGKM();
    float s1 = 0.f, dt = 0.f;
#pragma unroll
    for (int u = 0; u < 3; ++u) {
      const f32x4 a = acc[u] + *(const LAS f32x4*)(exw + L_EX + (u == 2 ? 2 : u + 3 * kh) * 1024);
      const unsigned a01 = cvtpk(a[0], a[1]), a23 = cvtpk(a[2], a[3]);
      hw[u][0] = cvtpk(bflo(xv[u][0]) + bflo(a01), bfhi(xv[u][0]) + bfhi(a01));
      hw[u][1] = cvtpk(bflo(xv[u][1]) + bflo(a23), bfhi(xv[u][1]) + bfhi(a23));
      const unsigned on = (u < 2 || keep2) ? 0x3f803f80u : 0u, w0 = (u < 2 || keep2) ? wv[u][0] : 0u, w1 = (u < 2 || keep2) ? wv[u][1] : 0u;
      if (u == 2 && !keep2) { hw[u][0] = 0u; hw[u][1] = 0u; }
      s1 = dot2(hw[u][0], on, dot2(hw[u][1], on, s1));
      dt = dot2(hw[u][0], w0, dot2(hw[u][1], w1, dt));
    }
    s1 = add32(add16(s1)); dt = add32(add16(dt));
    const float ml = s1 * (1.0f / 40.f);
    float m2 = 0.f;
#pragma unroll
    for (int u = 0; u < 3; ++u) {
      const float d0 = bflo(hw[u][0]) - ml, d1 = bfhi(hw[u][0]) - ml, d2 = bflo(hw[u][1]) - ml, d3 = bfhi(hw[u][1]) - ml;
      const float q = (d0 * d0 + d1 * d1) + (d2 * d2 + d3 * d3);
      m2 += (u < 2 || keep2) ? q : 0.f;
    }
    m2 = add32(add16(m2));
    LAS f32x4* st = (LAS f32x4*)(LW + L_ST2);
    if (g == 0) st[wid * 16 + c] = (f32x4){ml, m2, dt, 0.f};
    BAR_LGKM();
    if (wid == 0) {
      const f32x4 a = st[lane], bb = st[64 + lane];
      const float dm = a[0] - bb[0];
      const unsigned q0 = (__float_as_uint(0.5f * (a[0] + bb[0])) + 0x400u) >> 11, q1 = (__float_as_uint(a[1] + bb[1] + 20.f * dm * dm) + 0x400u) >> 11,
                     q2 = (__float_as_uint(a[2] + bb[2]) + 0x400u) >> 11;
      st_g64(ws + WS_ST + (size_t)(rg * 8 + hg) * 512 + lane * 8, q0 | (q1 << 21), (q1 >> 11) | (q2 << 10));
    }
  }
  STAMP(9);
  {
    const char* sp = ws + WS_ST + (size_t)(rg * 8 + wid) * 512 + lane * 8;
    u64 a;
    for (unsigned spins = 0; spins < SPIN_CAP; ++spins) {
      a = ld_g64(sp);
      if (__all(a != INVALID)) break;
      __builtin_amdgcn_s_sleep(PSLEEP);
    }
    LAS float* gs = (LAS float*)(LW + L_GS) + wid * 192 + lane;
    gs[0] = __uint_as_float((unsigned)a << 11); gs[64] = __uint_as_float(((unsigned)(a >> 21)) << 11); gs[128] = __uint_as_float(((unsigned)(a >> 42)) << 11);
  }
  sw = add32(add16(addrow(sw)));
  BAR_LGKM();
  STAMP(10);
  float mean_l, rstd_l, r_l; int best;
  {
    const LAS float* gs = (const LAS float*)(L + L_GS) + lane;
    float mu[8], mean = 0.f, M2 = 0.f, dt = 0.f;
#pragma unroll
    for (int i = 0; i < 8; ++i) { mu[i] = gs[i * 192]; mean += mu[i]; M2 += gs[i * 192 + 64]; dt += gs[i * 192 + 128]; }
    mean *= 0.125f;
#pragma unroll
    for (int i = 0; i < 8; ++i) { const float d = mu[i] - mean; M2 += 80.f * d * d; }
    mean_l = mean; rstd_l = __builtin_amdgcn_rsqf(M2 * (1.0f / D) + LN_EPS);
    r_l = rstd_l * (dt - mean * sw);
    const unsigned key = umax32(umax16(umaxrow((ordkey(r_l) & ~63u) | (unsigned)(63 - lane))));
    best = 63 - (int)(key & 63u);
  }
  if (hg == 0 && wid == 0) {
    const unsigned ck = (__builtin_amdgcn_readlane(ordkey(r_l), best) & ~2047u) | (unsigned)(2047 - (rg * 64 + best));
    if (lane < 32) st_g64(ws + WS_CAND + lane * 256 + rg * 8, ck, 0u);
  }
  const int lrow = mt * 16 + c;
  const float mean = __shfl(mean_l, lrow), rstd = __shfl(rstd_l, lrow);
  unsigned ow[3][2];
#pragma unroll
  for (int u = 0; u < 3; ++u) {
    const float h0 = bflo(hw[u][0]), h1 = bfhi(hw[u][0]), h2 = bflo(hw[u][1]), h3 = bfhi(hw[u][1]);
    const unsigned n01 = cvtpk((h0 - mean) * rstd, (h1 - mean) * rstd), n23 = cvtpk((h2 - mean) * rstd, (h3 - mean) * rstd);
    ow[u][0] = cvtpk(h0 + bflo(n01), h1 + bfhi(n01)); ow[u][1] = cvtpk(h2 + bflo(n23), h3 + bfhi(n23));
    if ((u < 2 || keep2) && lrow == best) {
      char* rw = ws + WS_ROW + rg * 2560 + (hg * 80 + colo[u]) * 2;
      st_val(rw, n01, n23); st_val(rw + 1280, hw[u][0], hw[u][1]);
    }
  }
  if (wid == 0) {
    u64 a;
    for (unsigned spins = 0; spins < SPIN_CAP; ++spins) {
      a = ld_g64(ws + WS_CAND + (b & 31) * 256 + r * 8);
      if (__all(a != INVALID)) break;
      __builtin_amdgcn_s_sleep(PSLEEP);
    }
    const unsigned key = umax16(umaxrow((unsigned)a));
    if (lane == 0) *(LAS unsigned*)(LW + L_RED + 1024) = key;
  }
  if (lrow != best) {
#pragma unroll
    for (int u = 0; u < 3; ++u)
      if (u < 2 || keep2) st_out((u32x2*)(p.out + (size_t)row * D + hg * 80 + colo[u]), (u32x2){ow[u][0], ow[u][1]});
  }
  STAMP(11);
  BAR_LGKM();
  const unsigned win = *(const LAS unsigned*)(L + L_RED + 1024);
  const unsigned wk_ = (win & ~2047u) | 1024u;
  const float r_sel = __uint_as_float((wk_ & 0x80000000u) ? (wk_ & 0x7fffffffu) : ~wk_);
  const int widx = 2047 - (int)(win & 2047u), wrg = widx >> 6;
  if (wrg != rg && lrow == best) {
#pragma unroll
    for (int u = 0; u < 3; ++u)
      if (u < 2 || keep2) st_out((u32x2*)(p.out + (size_t)row * D + hg * 80 + colo[u]), (u32x2){ow[u][0], ow[u][1]});
  }
  STAMP(12);
  if (hasG) {
    const char* rw = ws + WS_ROW + wrg * 2560;
    const int k0 = tid, k1 = tid < 128 ? tid + 512 : tid - 128, k2 = tid + 384 < 640 ? tid + 384 : 639;
    u32x4 fw[3];
#pragma unroll
    for (int j = 0; j < 3; ++j) { int e = tid + 512 * j; e = e < 1280 ? e : 1279; fw[j] = *(const LAS u32x4*)(L + L_FGU + e * 16); }
    u64 a0, a1, a2;
    for (unsigned spins = 0; spins < SPIN_CAP; ++spins) {
      a0 = ld_g64(rw + (k0 >> 2) * 8); a1 = ld_g64(rw + (k1 >> 2) * 8); a2 = ld_g64(rw + (k2 >> 2) * 8);
      if (__all((a0 != INVALID) & (a1 != INVALID) & (a2 != INVALID))) break;
      __builtin_amdgcn_s_sleep(PSLEEP);
    }
    const int sh = (tid & 3) * 16;
    const float x0 = bflo((unsigned)(a0 >> sh)), x1 = bflo((unsigned)(a1 >> sh)), x2 = tid < 256 ? bflo((unsigned)(a2 >> sh)) : 0.f;
    const float xg1 = tid < 128 ? x1 : 0.f, xu1 = tid < 128 ? 0.f : x1;
    float v8[8], v4[4];
#pragma unroll
    for (int j = 0; j < 4; ++j) {
      v8[2 * j] = rs32(x0 * bflo(fw[0][j]) + xg1 * bflo(fw[1][j]), xu1 * bflo(fw[1][j]) + x2 * bflo(fw[2][j]));
      v8[2 * j + 1] = rs32(x0 * bfhi(fw[0][j]) + xg1 * bfhi(fw[1][j]), xu1 * bfhi(fw[1][j]) + x2 * bfhi(fw[2][j]));
    }
#pragma unroll
    for (int j = 0; j < 4; ++j) v4[j] = addrow(rs16(v8[j], v8[4 + j]));
    LAS f32x4* red = (LAS f32x4*)(LW + L_RED);
    if (c == 0) red[wid * 4 + g] = (f32x4){v4[0], v4[1], v4[2], v4[3]};
    BAR_LGKM();
    if (wid == 0) {
      const LAS float* rf = (const LAS float*)(L + L_RED) + (lane & 7);
      float gv = 0.f, uv = 0.f;
#pragma unroll
      for (int w = 0; w < 8; ++w) { gv += rf[w * 16]; uv += rf[w * 16 + 8]; }
      gv = bfr(gv); uv = bfr(uv);
      const float act = bfr(gv * __builtin_amdgcn_rcpf(1.0f + __expf(-gv))) * uv;
      const unsigned pk = cvtpk(act, dppf<0xB1>(act));
      const unsigned pk2 = dppu<0x4E>(pk);
      if ((lane & 3) == 0) st_val(ws + WS_ACT + (lane >> 3) * 4096 + gsl * 16 + (lane & 4) * 2, pk, pk2);
    }
  }
  if (hasD) {
    const int tq = tid < 432 ? tid : 431;
    const int colb = dsl * 8;
    u32x4 fw[4];
#pragma unroll
    for (int j = 0; j < 4; ++j) fw[j] = *(const LAS u32x4*)(L + L_FD + (4 * tq + j) * 16);
    u64 a0, hn4, h4;
    if (!hasG) __builtin_amdgcn_s_sleep(ADELAY);
    for (unsigned spins = 0; spins < SPIN_CAP; ++spins) {
      a0 = ld_g64(ws + WS_ACT + xcd * 4096 + tq * 8);
      hn4 = ld_g64(ws + WS_ROW + wrg * 2560 + colb * 2 + (lane & 4) * 2); h4 = ld_g64(ws + WS_ROW + wrg * 2560 + 1280 + colb * 2 + (lane & 4) * 2);
      if (__all((a0 != INVALID) & (hn4 != INVALID) & (h4 != INVALID))) break;
      __builtin_amdgcn_s_sleep(ASLEEP);
    }
    float av[4] = {bflo((unsigned)a0), bfhi((unsigned)a0), bflo((unsigned)(a0 >> 32)), bfhi((unsigned)(a0 >> 32))};
    if (tid >= 432) { av[0] = av[1] = av[2] = av[3] = 0.f; }
    float y[8], v4[4], v2[2];
#pragma unroll
    for (int j = 0; j < 4; ++j) {
      y[2 * j] = (av[0] * bflo(fw[0][j]) + av[1] * bflo(fw[1][j])) + (av[2] * bflo(fw[2][j]) + av[3] * bflo(fw[3][j]));
      y[2 * j + 1] = (av[0] * bfhi(fw[0][j]) + av[1] * bfhi(fw[1][j])) + (av[2] * bfhi(fw[2][j]) + av[3] * bfhi(fw[3][j]));
    }
#pragma unroll
    for (int j = 0; j < 4; ++j) v4[j] = rs32(y[j], y[4 + j]);
#pragma unroll
    for (int j = 0; j < 2; ++j) v2[j] = addrow(rs16(v4[j], v4[2 + j]));
    LAS f32x2* red = (LAS f32x2*)(LW + L_RED + 512);
    if (c == 0) red[wid * 4 + g] = (f32x2){v2[0], v2[1]};
    BAR_LGKM();
    if (wid == 0 && lane < 8) {
      const LAS float* rf = (const LAS float*)(L + L_RED + 512) + lane;
      float tot = 0.f;
#pragma unroll
      for (int w = 0; w < 8; ++w) tot += rf[w * 8];
      const float rs = bfr(r_sel);
      const float sg = bfr(__builtin_amdgcn_rcpf(1.0f + __expf(-rs)));
      const float yv = bfr(bfr(tot) * sg);
      const float hv = bflo((unsigned)(h4 >> ((lane & 3) * 16))), hn = bflo((unsigned)(hn4 >> ((lane & 3) * 16)));
      p.out[(size_t)widx * D + colb + lane] = (u16)(cvtpk(hv + bfr(hn + yv), 0.f) & 0xffffu);
    }
  }
  }
  STAMP(13);
  STAMP_OUT();
}

extern "C" void kernel_launch(void* const* d_in, const int* in_sizes, int n_in, void* d_out, int out_size, void* d_ws, size_t ws_size, hipStream_t stream) {
  Params p;
  p.x = (const u16*)d_in[0]; p.wq = (const u16*)d_in[1]; p.wk = (const u16*)d_in[2]; p.wv = (const u16*)d_in[3]; p.wo = (const u16*)d_in[4];
  p.wr = (const u16*)d_in[6]; p.wg = (const u16*)d_in[7]; p.wu = (const u16*)d_in[8]; p.wd = (const u16*)d_in[9];
  p.out = (u16*)d_out; p.ws = (char*)d_ws;
  (void)hipFuncSetAttribute((const void*)fused_kernel, hipFuncAttributeMaxDynamicSharedMemorySize, LDS_BYTES);
  fused_kernel<<<256, 512, LDS_BYTES, stream>>>(p);
}
```

```cpp
#include <hip/hip_runtime.h>
#include <hip/hip_bf16.h>
#include <stdint.h>

typedef unsigned short u16;
typedef unsigned long long u64;
using bf16x8 = __attribute__((ext_vector_type(8))) short;
using bf16x4 = __attribute__((ext_vector_type(4))) short;
using f32x16 = __attribute__((ext_vector_type(16))) float;
using f32x4 = __attribute__((ext_vector_type(4))) float;
using f32x2 = __attribute__((ext_vector_type(2))) float;
using bfx2 = __attribute__((ext_vector_type(2))) __bf16;
using u32x4 = __attribute__((ext_vector_type(4))) unsigned;
using u32x2 = __attribute__((ext_vector_type(2))) unsigned;
#define LAS __attribute__((address_space(3)))
#define GAS __attribute__((address_space(1)))
typedef LAS const char* lds_cptr;
typedef LAS char* lds_ptr;
typedef GAS unsigned gu32;
typedef GAS u64 gu64;
#define RLX_AGENT __ATOMIC_RELAXED, __HIP_MEMORY_SCOPE_AGENT
#define SBAR() __builtin_amdgcn_sched_barrier(0)
#define PIN(x) asm volatile("" : "+v"(x))
#define MFMA16(a, b, c) __builtin_amdgcn_mfma_f32_32x32x16_bf16(a, b, c, 0, 0, 0)
#define MFMA8(a, b, c) __builtin_amdgcn_mfma_f32_32x32x8bf16_1k(a, b, c, 0, 0, 0)
#define MFMAS(a, b, c) __builtin_amdgcn_mfma_f32_16x16x32_bf16(a, b, c, 0, 0, 0)
#define DI __device__ __forceinline__
#define AS8(p) __builtin_bit_cast(bf16x8, p)
#define AS4(p) __builtin_bit_cast(bf16x4, p)

constexpr int S = 2048, D = 640, FF = 1728;
constexpr int TILE_B = 2624;
constexpr float QSCALE = 0.22360679774997896f * 1.4426950408889634f;
constexpr float LN_EPS = 1e-5f;
constexpr unsigned SPIN_CAP = 1u << 18;
#define PSLEEP 1
#define ADELAY 60
#define ASLEEP 2

constexpr size_t WS_EPOCH = 0;
constexpr size_t WS_F1 = 4096;
constexpr size_t WS_F2 = WS_F1 + 8 * 4096;
constexpr size_t WS_CAND = WS_F2 + 32 * 1024;
constexpr size_t WS_ACT = WS_CAND + 32 * 256;
constexpr size_t WS_ROW = WS_ACT + 8 * 4096;
constexpr size_t WS_ST = WS_ROW + 32 * 2560;
constexpr size_t WS_KV = WS_ST + 256 * 512;
constexpr size_t WS_O = WS_KV + 64 * TILE_B;
constexpr u64 INVALID = ~0ull;

constexpr int L_X = 0, L_WKV = 81920, L_WQA = L_WKV + 25664, L_SC = L_WQA + 51200;
constexpr int L_ST1 = 71680, L_CS = L_ST1 + 1024, L_MISC = L_CS + 640;
constexpr int L_WQB = 0, L_QX = 51200;
constexpr int L_WOA = 0;
constexpr int L_FGU = 51200;
constexpr int L_FD = L_FGU + 20480;
constexpr int L_OA = 99328;
constexpr int L_FL = 150528;
constexpr int L_Q = 151552;
constexpr int L_QB = L_Q + 8 * 1280;
constexpr int L_WOB = 99328;
constexpr int LDS_BYTES = 163840;
constexpr int L_EX = 0, L_ST2 = L_EX + 20480, L_GS = L_ST2 + 2048, L_RED = L_GS + 6144;

struct Params {
  const u16 *x, *wq, *wk, *wv, *wo, *wr, *wg, *wu, *wd;
  u16* out;
  char* ws;
};

DI unsigned cvtpk(float lo, float hi) { return __builtin_bit_cast(unsigned, __builtin_convertvector((f32x2){lo, hi}, bfx2)); }
DI float bflo(unsigned w) { return __uint_as_float(w << 16); }
DI float bfhi(unsigned w) { return __uint_as_float(w & 0xffff0000u); }
DI float bfr(float v) { return bflo(cvtpk(v, 0.f)); }
DI void glds(const void* sbase, unsigned voff, unsigned lds_dst) {
  unsigned sv;
  asm volatile("s_mov_b32 %0, m0\n\ts_mov_b32 m0, %3\n\ts_nop 0\n\tglobal_load_lds_dwordx4 %1, %2\n\ts_mov_b32 m0, %0" : "=&s"(sv) : "v"(voff), "s"(sbase), "s"(lds_dst) : "memory");
}
DI bf16x4 trread(lds_cptr p) { return AS4(__builtin_amdgcn_ds_read_tr16_b64_v4i16((LAS bf16x4*)p)); }
DI bf16x8 cat8(bf16x4 a, bf16x4 b) { return __builtin_shufflevector(a, b, 0, 1, 2, 3, 4, 5, 6, 7); }
DI float dot2(unsigned a, unsigned b, float c) { return __builtin_amdgcn_fdot2_f32_bf16(__builtin_bit_cast(bfx2, a), __builtin_bit_cast(bfx2, b), c, false); }
DI void st_g64(char* p, unsigned lo, unsigned hi) { __hip_atomic_store((gu64*)p, ((u64)hi << 32) | lo, RLX_AGENT); }
DI void st_out(u32x2* p, u32x2 v) { *p = v; }
DI void st_val(char* p, unsigned lo, unsigned hi) { st_g64(p, (lo & hi) == 0xffffffffu ? 0xfffffffeu : lo, hi); }
DI u64 ld_g64(const char* p) { return __hip_atomic_load((gu64*)p, RLX_AGENT); }
template <int CTRL> DI unsigned dppu(unsigned v) { return __builtin_amdgcn_update_dpp(0u, v, CTRL, 0xf, 0xf, true); }
template <int CTRL> DI float dppf(float v) { return __uint_as_float(dppu<CTRL>(__float_as_uint(v))); }
DI float add16(float v) { auto q = __builtin_amdgcn_permlane16_swap(__float_as_uint(v), __float_as_uint(v), false, false); return __uint_as_float(q[0]) + __uint_as_float(q[1]); }
DI float add32(float v) { auto q = __builtin_amdgcn_permlane32_swap(__float_as_uint(v), __float_as_uint(v), false, false); return __uint_as_float(q[0]) + __uint_as_float(q[1]); }
DI float addrow(float v) { v += dppf<0xB1>(v); v += dppf<0x4E>(v); v += dppf<0x141>(v); v += dppf<0x140>(v); return v; }
DI unsigned umax_(unsigned a, unsigned b) { return a > b ? a : b; }
DI unsigned umaxrow(unsigned v) { v = umax_(v, dppu<0xB1>(v)); v = umax_(v, dppu<0x4E>(v)); v = umax_(v, dppu<0x141>(v)); v = umax_(v, dppu<0x140>(v)); return v; }
DI unsigned umax16(unsigned v) { auto q = __builtin_amdgcn_permlane16_swap(v, v, false, false); return umax_(q[0], q[1]); }
DI unsigned umax32(unsigned v) { auto q = __builtin_amdgcn_permlane32_swap(v, v, false, false); return umax_(q[0], q[1]); }
DI float rs32(float a, float b) { auto q = __builtin_amdgcn_permlane32_swap(__float_as_uint(a), __float_as_uint(b), false, false); return __uint_as_float(q[0]) + __uint_as_float(q[1]); }
DI float rs16(float a, float b) { auto q = __builtin_amdgcn_permlane16_swap(__float_as_uint(a), __float_as_uint(b), false, false); return __uint_as_float(q[0]) + __uint_as_float(q[1]); }
DI unsigned ordkey(float f) { unsigned u = __float_as_uint(f); return (u & 0x80000000u) ? ~u : (u | 0x80000000u); }
#define BAR_LGKM() asm volatile("s_waitcnt lgkmcnt(0)\n\ts_barrier" ::: "memory")
#define DRAIN() asm volatile("s_waitcnt vmcnt(0)" ::: "memory")


__global__ __launch_bounds__(512, 2) void fused_kernel(Params p) {
  extern __shared__ __attribute__((aligned(16))) char lds[];
  char* ws = p.ws;
  const int tid = threadIdx.x, lane = tid & 63;
  const int wid = __builtin_amdgcn_readfirstlane(tid >> 6);
  const int c = lane & 15, g = lane >> 4;
  const int r = lane & 31, h = lane >> 5;
  const int mt = wid & 3, kh = wid >> 2;
  const int b = blockIdx.x, xcd = b & 7, slot = b >> 3, rg = xcd * 4 + (slot >> 3), hg = slot & 7;
  const int kvmt = hg >> 1, isV = hg & 1;
  const bool hasG = slot < 27, hasD = slot >= 22;
  const int gsl = xcd * 27 + slot, dsl = xcd * 10 + slot - 22;
  const bool pub = (wid == 0);
  const bool team = (wid < 4);
  const unsigned lds0 = (unsigned)(uintptr_t)lds;
  const lds_cptr L = (lds_cptr)lds;
  const lds_ptr LW = (lds_ptr)lds;
  const int q4 = c >> 2, p4 = c & 3;
  u32x4 xf[10];
  if (team) {
    const char* xs_ = (const char*)p.x + (size_t)(rg * 64 + kvmt * 16) * 1280 + wid * 320;
    const unsigned vo = c * 1280 + g * 16;
    asm volatile("global_load_dwordx4 %0, %5, %6\n\tglobal_load_dwordx4 %1, %5, %6 offset:64\n\tglobal_load_dwordx4 %2, %5, %6 offset:128\n\t"
                 "global_load_dwordx4 %3, %5, %6 offset:192\n\tglobal_load_dwordx4 %4, %5, %6 offset:256"
                 : "=&v"(xf[0]), "=&v"(xf[1]), "=&v"(xf[2]), "=&v"(xf[3]), "=&v"(xf[4]) : "v"(vo), "s"(xs_) : "memory");
    const char* src = (const char*)(isV ? p.wv : p.wk) + wid * 6400;
#pragma unroll
    for (int i = 0; i < 6; ++i) glds(src + i * 1024, lane * 16, lds0 + L_WKV + wid * 6400 + i * 1024);
    if (lane < 16) glds(src + 6144, lane * 16, lds0 + L_WKV + wid * 6400 + 6144);
  } else {
    const unsigned voff_x = g * 1280 + ((c ^ g ^ (4 * mt)) * 16);
#pragma unroll
    for (int m = 0; m < 4; ++m) {
      const char* src = (const char*)p.x + (size_t)(rg * 64 + 16 * m + 4 * mt) * 1280;
#pragma unroll
      for (int sg = 0; sg < 5; ++sg) glds(src + sg * 256, voff_x, lds0 + L_X + ((4 * m + mt) * 5 + sg) * 1024);
    }
  }
  unsigned voffw[5];
#pragma unroll
  for (int ph = 0; ph < 5; ++ph) { const int ci = ph * 64 + lane, rw = ci / 10; voffw[ph] = rw * 1280 + (ci - rw * 10) * 16; }
  auto dma_wgrp = [&](const u16* w, int row0, unsigned dst) {
    const char* src = (const char*)w + (size_t)row0 * 1280 + hg * 160;
#pragma unroll
    for (int ph = 0; ph < 5; ++ph) glds(src, voffw[ph], dst + ph * 1024);
  };
  SBAR();
  unsigned tag = 0;
  if (pub) tag = __hip_atomic_load((gu32*)(ws + WS_EPOCH), RLX_AGENT) + 1u;
  const int ro1 = 8 * g + (g & 1) * 4 + q4, ro2 = 8 * g + (1 - (g & 1)) * 4 + q4;
  bf16x8 ones = AS8(((u32x4){0x3f803f80u, 0x3f803f80u, 0x3f803f80u, 0x3f803f80u}));
  PIN(ones);
  auto diag = [&](const f32x4& gr) { return p4 == 0 ? gr[0] : p4 == 1 ? gr[1] : p4 == 2 ? gr[2] : gr[3]; };
  if (team) {
    if (wid == 1) {
      st_g64(ws + WS_ST + (size_t)(rg * 8 + hg) * 512 + lane * 8, ~0u, ~0u);
      char* a1 = lane < 40 ? ws + WS_ROW + rg * 2560 + (lane < 20 ? 0 : 1280 - 160) + hg * 160 + lane * 8
               : lane < 48 ? ws + WS_F2 + rg * 1024 + (lane - 40) * 128 + hg * 8 : ws + WS_ACT + ((lane - 48) >> 1) * 4096 + gsl * 16 + (lane & 1) * 8;
      if (lane < 48 || hasG) st_g64(a1, ~0u, ~0u);
      if (hg == 0 && lane < 32) st_g64(ws + WS_CAND + lane * 256 + rg * 8, ~0u, ~0u);
    }
    unsigned k1 = L_WKV + wid * 6400 + (8 * g + q4) * 40 + p4 * 8;
    unsigned pw = L_WKV + wid * 6400 + (isV ? (4 * g * 16 + c) * 4 : (c * 24 + 4 * g) * 4);
    PIN(k1); PIN(pw);
    asm volatile("s_waitcnt vmcnt(0)" : "+v"(xf[0]), "+v"(xf[1]), "+v"(xf[2]), "+v"(xf[3]), "+v"(xf[4]) :: "memory");
    f32x4 ax0 = {}, ax1 = {}, cx0 = {}, cx1 = {}, sm = {}, gr = {};
    bf16x8 wf[5][2];
#pragma unroll
    for (int s = 0; s < 5; ++s)
#pragma unroll
      for (int u = 0; u < 2; ++u) wf[s][u] = cat8(trread(L + k1 + s * 1280 + u * 32), trread(L + k1 + s * 1280 + u * 32 + 160));
#pragma unroll
    for (int s = 0; s < 5; ++s) { sm = MFMAS(ones, AS8(xf[s]), sm); gr = MFMAS(AS8(xf[s]), AS8(xf[s]), gr); }
#pragma unroll
    for (int s = 0; s < 5; ++s) {
      ax0 = MFMAS(wf[s][0], AS8(xf[s]), ax0); ax1 = MFMAS(wf[s][1], AS8(xf[s]), ax1);
      cx0 = MFMAS(wf[s][0], ones, cx0); cx1 = MFMAS(wf[s][1], ones, cx1);
    }
    SBAR();
    const lds_ptr pb = LW + L_WKV + wid * 6400;
    if (g == q4) *(LAS f32x2*)(pb + 1536 + c * 8) = (f32x2){sm[0], diag(gr)};
    if (c == 0) { *(LAS f32x4*)(pb + 1664 + g * 16) = cx0; *(LAS f32x4*)(pb + 1664 + 64 + g * 16) = cx1; }
    if (isV) {
#pragma unroll
      for (int i = 0; i < 4; ++i) *(LAS float*)(LW + pw + i * 64) = ax0[i];
      if (g == 0) {
#pragma unroll
        for (int i = 0; i < 4; ++i) *(LAS float*)(LW + pw + 1024 + i * 64) = ax1[i];
      }
    } else {
      *(LAS f32x4*)(LW + pw) = ax0;
      if (g == 0) *(LAS f32x4*)(LW + pw + 64) = ax1;
    }
  } else {
#pragma unroll
    for (int j = 0; j < 3; ++j) {
      const int idx = mt + 4 * j, gi = (idx & 1) * 5 + (idx >> 1);
      if (idx < 10) dma_wgrp(p.wq, (idx & 1) * 320 + (idx >> 1) * 32, lds0 + L_WQA + gi * 5120);
    }
    asm volatile("s_waitcnt vmcnt(10)" ::: "memory");
  }
  unsigned xa[4];
#pragma unroll
  for (int u = 0; u < 4; ++u) { xa[u] = L_X + (4 * mt + q4) * 5120 + p4 * 256 + ((g ^ p4) * 16) + (((4 * ((u + 2 * kh) & 3)) ^ (c & 12)) * 16) + (g & 1) * 8 + ((u + 10 * kh) >> 2) * 1024; PIN(xa[u]); }
  BAR_LGKM();
#pragma unroll
  for (int s = 0; s < 10; ++s) {
    const unsigned a_ = xa[s & 3] + ((s >> 2) * 1024);
    const u32x2 a = *(const LAS u32x2*)(L + a_), bb = *(const LAS u32x2*)(L + (a_ ^ 8));
    xf[s] = (u32x4){a[0], a[1], bb[0], bb[1]};
  }
  BAR_LGKM();
  float kn2 = 0.f;
  if (pub) {
    const lds_cptr pb = L + L_WKV;
    const int gm = rg * 4 + kvmt;
    char* tb = ws + WS_KV + (size_t)(gm >> 1) * TILE_B;
    auto kv_stats = [&](int pp, float& mean, float& rstd) {
      const f32x2 a = (*(const LAS f32x2*)(pb + 1536 + pp * 8) + *(const LAS f32x2*)(pb + 6400 + 1536 + pp * 8)) +
                      (*(const LAS f32x2*)(pb + 12800 + 1536 + pp * 8) + *(const LAS f32x2*)(pb + 19200 + 1536 + pp * 8));
      mean = a[0] * (1.0f / D);
      rstd = __builtin_amdgcn_rsqf(fmaxf(a[1] * (1.0f / D) - mean * mean, 0.f) + LN_EPS);
    };
    if (!isV) {
      float mean, rstd; kv_stats(c, mean, rstd);
      f32x4 a0 = {}, a1 = {}, c0 = {}, c1 = {};
#pragma unroll
      for (int w = 0; w < 4; ++w) {
        a0 += *(const LAS f32x4*)(pb + w * 6400 + (c * 24 + 4 * g) * 4); a1 += *(const LAS f32x4*)(pb + w * 6400 + (c * 24 + 16) * 4);
        c0 += *(const LAS f32x4*)(pb + w * 6400 + 1664 + g * 16); c1 += *(const LAS f32x4*)(pb + w * 6400 + 1664 + 64);
      }
      const unsigned w0 = cvtpk((a0[0] - mean * c0[0]) * rstd, (a0[1] - mean * c0[1]) * rstd), w1 = cvtpk((a0[2] - mean * c0[2]) * rstd, (a0[3] - mean * c0[3]) * rstd);
      const unsigned w2 = cvtpk((a1[0] - mean * c1[0]) * rstd, (a1[1] - mean * c1[1]) * rstd), w3 = cvtpk((a1[2] - mean * c1[2]) * rstd, (a1[3] - mean * c1[3]) * rstd);
      const int rr = (gm & 1) * 16 + c;
      st_g64(tb + ((g >> 1) * 32 + rr) * 16 + (g & 1) * 8, w0, w1);
      if (g == 0) st_g64(tb + 1024 + rr * 8, w2, w3);
      const float n2 = add32(add16(dot2(w0, w0, dot2(w1, w1, 0.f)))) + dot2(w2, w2, dot2(w3, w3, 0.f));
      kn2 = __uint_as_float(umaxrow(__float_as_uint(n2)));
    } else {
      f32x4 a0 = {}, a1 = {}; float cs0 = 0.f, cs1 = 0.f;
#pragma unroll
      for (int w = 0; w < 4; ++w) {
        a0 += *(const LAS f32x4*)(pb + w * 6400 + (c * 16 + 4 * g) * 4); a1 += *(const LAS f32x4*)(pb + w * 6400 + ((16 + p4) * 16 + 4 * g) * 4);
        cs0 += *(const LAS float*)(pb + w * 6400 + 1664 + c * 4); cs1 += *(const LAS float*)(pb + w * 6400 + 1664 + 64 + p4 * 4);
      }
      float v0[4], v1[4];
#pragma unroll
      for (int i = 0; i < 4; ++i) {
        float mean, rstd; kv_stats(4 * g + i, mean, rstd);
        v0[i] = (a0[i] - mean * cs0) * rstd; v1[i] = (a1[i] - mean * cs1) * rstd;
      }
      char* vb = tb + 1280 + (((gm & 1) * 2 + (g & 1)) * 21) * 16 + (g >> 1) * 8;
      st_g64(vb + c * 16, cvtpk(v0[0], v0[1]), cvtpk(v0[2], v0[3]));
      if (c < 5) st_g64(vb + (16 + c) * 16, c < 4 ? cvtpk(v1[0], v1[1]) : 0x3f803f80u, c < 4 ? cvtpk(v1[2], v1[3]) : 0x3f803f80u);
    }
  } else {
    const int w7 = wid - 1;
    dma_wgrp(p.wq, (w7 & 1) * 320 + (w7 >> 1) * 32 + 160, lds0 + L_WQB + ((w7 & 1) * 5 + (w7 >> 1)) * 5120);
    if (w7 < 3) { const int idx = w7 + 7; dma_wgrp(p.wq, (idx & 1) * 320 + (idx >> 1) * 32 + 160, lds0 + L_WQB + ((idx & 1) * 5 + (idx >> 1)) * 5120); }
  }
  {
    f32x4 sm = {}, gr = {};
#pragma unroll
    for (int s = 0; s < 10; ++s) { sm = MFMAS(ones, AS8(xf[s]), sm); gr = MFMAS(AS8(xf[s]), AS8(xf[s]), gr); }
    if (g == q4) *(LAS f32x2*)(LW + L_ST1 + (wid * 16 + c) * 8) = (f32x2){sm[0], diag(gr)};
  }
  SBAR();
  asm volatile("s_waitcnt vmcnt(5)\n\ts_barrier" ::: "memory");
  if (pub) {
    DRAIN();
    if (lane < 16) st_g64(ws + WS_F1 + (lane >> 1) * 4096 + b * 16 + (lane & 1) * 8, (lane & 1) == 0 ? __float_as_uint(kn2) : ~tag ^ 0x5a5a5a5au, tag);
  }
  const unsigned wA1 = (160 * kh + ro1) * 160 + p4 * 8, wA2 = (160 * kh + ro2) * 160 + p4 * 8;
  f32x4 acc[5] = {}, csa = {}, csb = {};
  bf16x8 kl[8], va[8], vb[8]; bf16x4 kx[8];
  const __amdgpu_buffer_rsrc_t rskv = __builtin_amdgcn_make_buffer_rsrc((void*)(ws + WS_KV), 0, 64 * TILE_B, 0x00020000);
  const unsigned kvb = wid * 8 * TILE_B;
  const unsigned oKA = kvb + lane * 16, oKB = kvb + (h == 0 ? 1024 + r * 8 : 1280 + 20 * 16);
  const unsigned oV = r <= 20 ? kvb + 1280 + (h * 21 + r) * 16 : 0x40000000u;
#define LOAD_KV(i) asm volatile("buffer_load_dwordx4 %0, %4, %7, %8 offen sc1\n\tbuffer_load_dwordx2 %1, %5, %7, %8 offen sc1\n\t" \
      "buffer_load_dwordx4 %2, %6, %7, %8 offen sc1\n\tbuffer_load_dwordx4 %3, %6, %7, %8 offen offset:672 sc1" \
      : "+v"(kl[i]), "+v"(kx[i]), "+v"(va[i]), "+v"(vb[i]) : "v"(oKA), "v"(oKB), "v"(oV), "s"(rskv), "s"((i) * TILE_B) : "memory")
#define LOAD_KV_ALL() do { LOAD_KV(0); LOAD_KV(1); LOAD_KV(2); LOAD_KV(3); LOAD_KV(4); LOAD_KV(5); LOAD_KV(6); LOAD_KV(7); } while (0)
#define RDW(dst, o1, o2, off) dst = cat8(trread(L + (o1) + (off)), trread(L + (o2) + (off)))
#define RDU(buf, s, u) do { \
      if ((u) == 0) RDW(wf[buf][0], a1, a2, (s) * 5120); if ((u) == 1) RDW(wf[buf][1], a1, a2, (s) * 5120 + 32); if ((u) == 2) RDW(wf[buf][2], b1, b2, (s) * 5120); \
      if ((u) == 3) RDW(wf[buf][3], c1, c2, (s) * 5120); if ((u) == 4) RDW(wf[buf][4], c1, c2, (s) * 5120 + 32); \
      if ((u) == 5) RDW(wf[buf][5], d1, d2, (s) * 5120); if ((u) == 6) RDW(wf[buf][6], b1, b2, (s) * 5120 + 64); } while (0)
#define PSTEPS(base, XF, s0, CS, KV) do { \
    const unsigned a1 = (base) + wA1 + 96 * kh, a2 = (base) + wA2 + 96 * kh, b1 = (base) + wA1 + 64, b2 = (base) + wA2 + 64; \
    const unsigned c1 = (base) + wA1 + 96 - 96 * kh, c2 = (base) + wA2 + 96 - 96 * kh, d1 = (base) + wA1 + 32 * mt, d2 = (base) + wA2 + 32 * mt; \
    bf16x8 wf[2][7]; \
    _Pragma("unroll") for (int u = 0; u < ((CS) ? 7 : 5); ++u) RDU(0, 0, u); \
    SBAR(); \
    _Pragma("unroll") for (int s = 0; s < 5; ++s) { \
      const bf16x8 xs = AS8(XF[(s0) + s]); \
      _Pragma("unroll") for (int u = 0; u < ((CS) ? 7 : 5); ++u) { \
        if (u < 5) { acc[u] = MFMAS(wf[s & 1][u], xs, acc[u]); PIN(acc[u]); } \
        else if (u == 5) { csa = MFMAS(wf[s & 1][5], ones, csa); PIN(csa); } \
        else { csb = MFMAS(wf[s & 1][6], ones, csb); PIN(csb); } \
        SBAR(); \
        if (s + 1 < 5) RDU((s + 1) & 1, s + 1, u); \
        if ((KV) && s < 4 && u == 0 && kh == 0) { LOAD_KV(2 * (s & 3)); LOAD_KV(2 * (s & 3) + 1); }     \
        SBAR(); \
      } \
    } } while (0)
  PSTEPS(L_WQA, xf, 0, true, false);
  unsigned qdst[3];
#pragma unroll
  for (int u = 0; u < 3; ++u) {
    const int t = u == 2 ? 2 : u + 3 * kh, n = 16 * t + 4 * g, hh = (n * 205) >> 12, d = n - hh * 20, rr = (mt & 1) * 16 + c;
    qdst[u] = L_Q + (hh * 2 + (mt >> 1)) * 1280 + (d < 16 ? ((d >> 3) * 32 + rr) * 16 + (d & 4) * 2 : 1024 + rr * 8);
    PIN(qdst[u]);
  }
  if (pub) {
    unsigned km = 0;
    for (unsigned spins = 0; spins < SPIN_CAP; ++spins) {
      bool ok = true; km = 0;
#pragma unroll
      for (int i = 0; i < 4; ++i) {
        const u64 a = ld_g64(ws + WS_F1 + xcd * 4096 + (i * 64 + lane) * 16), bb = ld_g64(ws + WS_F1 + xcd * 4096 + (i * 64 + lane) * 16 + 8);
        ok &= ((unsigned)(a >> 32) == tag) & ((unsigned)(bb >> 32) == tag) & ((unsigned)bb == (~tag ^ 0x5a5a5a5au));
        km = umax_(km, (unsigned)a);
      }
      if (__all(ok)) break;
      __builtin_amdgcn_s_sleep(1);
    }
    km = umax32(umax16(umaxrow(km)));
    if (lane == 0) *(LAS unsigned*)(LW + L_MISC) = km;
    if (b == 0 && lane == 0) __hip_atomic_store((gu32*)(ws + WS_EPOCH), tag, RLX_AGENT);
  } else {
    DRAIN();
  }
  BAR_LGKM();
  if (tid < 17) *(LAS unsigned*)(LW + L_FL + tid * 4) = 0u;
#pragma unroll
  for (int i = 0; i < 8; ++i) asm volatile("" : "=v"(kl[i]), "=v"(kx[i]), "=v"(va[i]), "=v"(vb[i]));
  PSTEPS(L_WQB, xf, 5, true, true);
  const bool keep2 = (g >> 1) == kh;
  const lds_ptr exw = LW + mt * 5120 + lane * 16;
  {
    LAS float* csp = (LAS float*)(LW + L_CS);
    if (c == 0) {
      *(LAS f32x4*)(csp + (kh * 5 + mt) * 16 + 4 * g) = csa;
      if (mt == 0) *(LAS f32x4*)(csp + (kh * 5 + 4) * 16 + 4 * g) = csb;
    }
    *(LAS f32x4*)(exw + L_QX + 3072 * (1 - kh)) = acc[3]; *(LAS f32x4*)(exw + L_QX + 3072 * (1 - kh) + 1024) = acc[4];
    if (!keep2) *(LAS f32x4*)(exw + L_QX + 2048) = acc[2];
    const float km2 = *(const LAS float*)(L + L_MISC);
    const f32x2 sa = *(const LAS f32x2*)(L + L_ST1 + (mt * 16 + c) * 8), sb = *(const LAS f32x2*)(L + L_ST1 + ((mt + 4) * 16 + c) * 8);
    float mean = (sa[0] + sb[0]) * (1.0f / D);
    float rstd = __builtin_amdgcn_rsqf(fmaxf((sa[1] + sb[1]) * (1.0f / D) - mean * mean, 0.f) + LN_EPS) * QSCALE;
    PIN(mean); PIN(rstd);
    BAR_LGKM();
    f32x4 pa[3], c0[3], c1[3];
#pragma unroll
    for (int u = 0; u < 3; ++u) {
      const int t = u == 2 ? 2 : u + 3 * kh;
      pa[u] = *(const LAS f32x4*)(exw + L_QX + t * 1024);
      c0[u] = *(const LAS f32x4*)(csp + t * 16 + 4 * g); c1[u] = *(const LAS f32x4*)(csp + (5 + t) * 16 + 4 * g);
    }
#pragma unroll
    for (int u = 0; u < 3; ++u) {
      const f32x4 a = acc[u] + pa[u], cv = c0[u] + c1[u];
      const u32x2 qv = (u32x2){cvtpk((a[0] - mean * cv[0]) * rstd, (a[1] - mean * cv[1]) * rstd), cvtpk((a[2] - mean * cv[2]) * rstd, (a[3] - mean * cv[3]) * rstd)};
      if (u < 2 || keep2) *(LAS u32x2*)(LW + qdst[u]) = qv;
    }
    BAR_LGKM();
    const u32x4 ql = *(const LAS u32x4*)(L + L_Q + wid * 1280 + lane * 16);
    const u32x2 qh = *(const LAS u32x2*)(L + L_Q + wid * 1280 + 1024 + r * 8);
    float n2 = dot2(ql[0], ql[0], dot2(ql[1], ql[1], 0.f)) + dot2(ql[2], ql[2], dot2(ql[3], ql[3], 0.f));
    if (h == 0) n2 += dot2(qh[0], qh[0], dot2(qh[1], qh[1], 0.f));
    n2 = add32(n2);
    const float M = __builtin_amdgcn_sqrtf(n2 * km2) * 1.0078125f;
    if (h == 0) *(LAS u32x2*)(LW + L_QB + wid * 256 + r * 8) = (u32x2){cvtpk(-M, 0.f), 0u};
  }
  const unsigned oQL = L_Q + lane * 16, oQH = h == 0 ? L_Q + 1024 + r * 8 : L_QB + r * 8;
  const unsigned sQH = h == 0 ? 1280 : 256;
  volatile LAS unsigned* fl = (volatile LAS unsigned*)(LW + L_FL);
#pragma unroll 1
  for (int pass = 0; pass < 2; ++pass) {
    BAR_LGKM();
    if (pass == 0 && kh == 1) LOAD_KV_ALL();
    f32x16 OA = {}, OB = {}, SA, SB;
    u32x4 p0, p1 = {0, 0, 0, 0};
    bf16x8 qcl, qnl; bf16x4 qch, qnh;
    unsigned flv;
    qcl = *(const LAS bf16x8*)(L + oQL + 2 * mt * 1280);
    qch = *(const LAS bf16x4*)(L + oQH + 2 * mt * sQH);
    DRAIN();
    if (kh == 0) __builtin_amdgcn_s_setprio(2); else __builtin_amdgcn_s_setprio(1);
    SBAR();
    SA = MFMA8(kx[0], qch, MFMA16(kl[0], qcl, (f32x16){}));
    SBAR();
#define EXG(X, i, P, j) do { X[i] = __builtin_amdgcn_exp2f(X[i]); X[i + 1] = __builtin_amdgcn_exp2f(X[i + 1]); P[j] = cvtpk(X[i], X[i + 1]); \
                             X[i + 2] = __builtin_amdgcn_exp2f(X[i + 2]); X[i + 3] = __builtin_amdgcn_exp2f(X[i + 3]); P[j + 1] = cvtpk(X[i + 2], X[i + 3]); PIN(P); SBAR(); } while (0)
#define STEP(C, N, i, OP, OC, QL, QH, EXTRA) do { \
      N = MFMA16(kl[((i) + 1) & 7], QL, (f32x16){}); PIN(N); SBAR(); \
      EXG(C, 0, p0, 0); \
      OP = MFMA16(vb[((i) + 7) & 7], AS8(p1), OP); PIN(OP); SBAR(); \
      EXG(C, 4, p0, 2); \
      N = MFMA8(kx[((i) + 1) & 7], QH, N); PIN(N); SBAR(); \
      EXTRA; \
      EXG(C, 8, p1, 0); \
      OC = MFMA16(va[i], AS8(p0), OC); PIN(OC); SBAR(); \
      EXG(C, 12, p1, 2); \
    } while (0)
#define WR_O(X, tt) do { const int sl_ = kh * 8 + ((2 * mt + (tt) + 7) & 7); lds_ptr d_ = LW + L_OA + sl_ * 3072 + lane * 16; \
      *(LAS f32x4*)(d_) = (f32x4){X[0], X[1], X[2], X[3]}; *(LAS f32x4*)(d_ + 1024) = (f32x4){X[4], X[5], X[6], X[7]}; \
      *(LAS f32x4*)(d_ + 2048) = (f32x4){X[8], X[9], X[10], X[11]}; asm volatile("" ::: "memory"); \
      fl[sl_] = pass * 16 + (tt) + 1; PIN(X); SBAR(); } while (0)
#define LD_O(X, sl_) do { lds_cptr s_ = L + L_OA + (sl_) * 3072 + lane * 16; \
      const f32x4 a_ = *(const LAS f32x4*)(s_), b_ = *(const LAS f32x4*)(s_ + 1024), c_ = *(const LAS f32x4*)(s_ + 2048); \
      X[0] = a_[0]; X[1] = a_[1]; X[2] = a_[2]; X[3] = a_[3]; X[4] = b_[0]; X[5] = b_[1]; X[6] = b_[2]; X[7] = b_[3]; \
      X[8] = c_[0]; X[9] = c_[1]; X[10] = c_[2]; X[11] = c_[3]; } while (0)
#define RSLOT() (t < 7 ? kh * 8 + ((2 * mt + t + 1) & 7) : ((2 * mt + 6 + kh) & 7))
#define RWANT() ((unsigned)(pass * 16 + t + 1 + ((t == 7) ? kh : 0)))
#define RD_O(X) do { const int sl_ = RSLOT(); flv = fl[sl_]; asm volatile("" ::: "memory"); LD_O(X, sl_); SBAR(); } while (0)
#define CK_O(X) do { if (__builtin_expect(__builtin_amdgcn_readfirstlane(flv) != RWANT(), 0)) { \
        const int sl_ = RSLOT(); \
        while (fl[sl_] != RWANT()) __builtin_amdgcn_s_sleep(1); \
        asm volatile("" ::: "memory"); LD_O(X, sl_); } SBAR(); } while (0)
#define RD_Q() do { const int jq_ = (2 * mt + t + 1) & 7; \
      qnl = *(const LAS bf16x8*)(L + oQL + jq_ * 1280); qnh = *(const LAS bf16x4*)(L + oQH + jq_ * sQH); SBAR(); } while (0)
#define ROUND(OC, OP) do { \
      STEP(SA, SB, 0, OP, OC, qcl, qch, ); \
      STEP(SB, SA, 1, OC, OC, qcl, qch, WR_O(OP, t)); \
      STEP(SA, SB, 2, OC, OC, qcl, qch, ); \
      STEP(SB, SA, 3, OC, OC, qcl, qch, RD_Q()); \
      STEP(SA, SB, 4, OC, OC, qcl, qch, ); \
      STEP(SB, SA, 5, OC, OC, qcl, qch, RD_O(OP)); \
      STEP(SA, SB, 6, OC, OC, qcl, qch, ); \
      STEP(SB, SA, 7, OC, OC, qnl, qnh, CK_O(OP)); \
      qcl = qnl; qch = qnh; \
    } while (0)
#pragma unroll 1
    for (int t = 0; t < 8; ++t) {
      ROUND(OA, OB);
      ++t;
      ROUND(OB, OA);
    }
    OB = MFMA16(vb[7], AS8(p1), OB);
    __builtin_amdgcn_s_setprio(0);
    if (kh == 0) {
      WR_O(OB, 8);
      if (pass == 0) {
        int ln = lane; PIN(ln);
        unsigned vw[5];
#pragma unroll
        for (int ph = 0; ph < 5; ++ph) { const int ci = ph * 64 + ln, rw = ci / 10; vw[ph] = rw * 1280 + (ci - rw * 10) * 16; }
#pragma unroll 1
        for (int gj = mt; gj < 10; gj += 4) {
          const char* src = (const char*)p.wo + (size_t)(gj < 5 ? gj * 32 : 320 + (gj - 5) * 32) * 1280 + hg * 160;
#pragma unroll
          for (int ph = 0; ph < 5; ++ph) glds(src, vw[ph], lds0 + L_WOA + gj * 5120 + ph * 1024);
        }
        if (hasG) {
#pragma unroll 1
          for (int f = mt; f < 20; f += 4)
            glds((const char*)(f < 10 ? p.wg : p.wu) + (size_t)(f < 10 ? f : f - 10) * (64 * FF * 2) + gsl * 16, ln * (FF * 2), lds0 + L_FGU + f * 1024);
        }
        if (hasD) {
#pragma unroll 1
          for (int d = mt; d < 27; d += 4) glds((const char*)p.wd + (size_t)d * (64 * 1280) + dsl * 16, ln * 1280, lds0 + L_FD + d * 1024);
        }
      }
      const int sl = 8 + ((2 * mt + 6) & 7);
      while (fl[sl] != (unsigned)(pass * 16 + 8)) __builtin_amdgcn_s_sleep(4);
      asm volatile("" ::: "memory");
      LD_O(OB, sl);
      LD_O(OA, sl - 8);
    }
    float F[12];
#pragma unroll
    for (int i = 0; i < 12; ++i) F[i] = OB[i] + OA[i];
    auto lq = __builtin_amdgcn_permlane32_swap(__float_as_uint(F[8]), __float_as_uint(F[8]), false, false);
    const float l = __uint_as_float(lq[1]);
    if (pass == 0 && __any(!(l >= 5.421010862427522e-20f))) fl[16] = 1u;
    {
      const int j = (2 * mt + 6 + kh) & 7, pos = (j & 1) * 32 + r;
      const float rl = __builtin_amdgcn_rcpf(l);
      char* ob = ws + WS_O + (size_t)rg * 81920 + (size_t)(pos >> 4) * 20480 + (pos & 15) * 16;
      const int col0 = hg * 80 + (j >> 1) * 20;
      auto put = [&](int d, float a0, float a1, float a2, float a3) {
        const int col = col0 + d, oc = col >> 3, gg = oc & 3, half = ((col >> 2) & 1) ^ (gg & 1);
        st_g64(ob + (oc >> 2) * 1024 + gg * 256 + half * 8, cvtpk(a0 * rl, a1 * rl), cvtpk(a2 * rl, a3 * rl));
      };
      put(4 * h, F[0], F[1], F[2], F[3]);
      put(8 + 4 * h, F[4], F[5], F[6], F[7]);
      if (h == 0) put(16, F[8], F[9], F[10], F[11]);
    }
    asm volatile("s_waitcnt vmcnt(0) lgkmcnt(0)\n\ts_barrier" ::: "memory");
    if (__builtin_expect(pass == 1 || fl[16] == 0u, 1)) break;
    {
      LAS float* mx = (LAS float*)(LW + L_OA);
#pragma unroll 1
      for (int j = 0; j < 8; ++j) {
        const bf16x8 ql = *(const LAS bf16x8*)(L + oQL + j * 1280);
        bf16x4 qh = *(const LAS bf16x4*)(L + L_Q + 1024 + r * 8 + j * 1280);
        if (h == 1) qh = (bf16x4){};
        float m = -INFINITY;
#pragma unroll
        for (int i = 0; i < 8; ++i) {
          const f32x16 sc = MFMA8(kx[i], qh, MFMA16(kl[i], ql, (f32x16){}));
#pragma unroll
          for (int e = 0; e < 16; ++e) m = fmaxf(m, sc[e]);
        }
        m = fmaxf(m, __shfl_xor(m, 32));
        if (h == 0) mx[(wid * 8 + j) * 32 + r] = m;
      }
      BAR_LGKM();
      float m = -INFINITY;
#pragma unroll
      for (int w = 0; w < 8; ++w) m = fmaxf(m, mx[(w * 8 + wid) * 32 + r]);
      const float nm = -m, a = bfr(nm), r1 = nm - a, bb = bfr(r1), cc = r1 - bb;
      if (h == 0) *(LAS u32x2*)(LW + L_QB + wid * 256 + r * 8) = (u32x2){cvtpk(a, bb), cvtpk(cc, 0.f)};
    }
  }
  if (tid < 8) st_g64(ws + WS_F2 + rg * 1024 + tid * 128 + hg * 8, 1u, 0u);
  int lane_t = lane; PIN(lane_t);
  {
  const int lane = lane_t, tid = wid * 64 + lane, c = lane & 15, g = lane >> 4, r = lane & 31;
  const int q4 = c >> 2, p4 = c & 3;
  const unsigned wA1 = (160 * kh + 8 * g + (g & 1) * 4 + q4) * 160 + p4 * 8, wA2 = (160 * kh + 8 * g + (1 - (g & 1)) * 4 + q4) * 160 + p4 * 8;
  const bool keep2 = (g >> 1) == kh;
  const lds_ptr exw = LW + mt * 5120 + lane * 16;
  {
    unsigned vw[5];
#pragma unroll
    for (int ph = 0; ph < 5; ++ph) { const int ci = ph * 64 + lane, rw = ci / 10; vw[ph] = rw * 1280 + (ci - rw * 10) * 16; }
    const char* src = (const char*)p.wo + (size_t)((wid & 1) * 320 + (wid >> 1) * 32 + 160) * 1280 + hg * 160;
#pragma unroll
    for (int ph = 0; ph < 5; ++ph) glds(src, vw[ph], lds0 + L_WOB + ((wid & 1) * 5 + (wid >> 1)) * 5120 + ph * 1024);
    if (wid < 2) {
      src = (const char*)p.wo + (size_t)(wid * 320 + 128 + 160) * 1280 + hg * 160;
#pragma unroll
      for (int ph = 0; ph < 5; ++ph) glds(src, vw[ph], lds0 + L_WOB + (wid * 5 + 4) * 5120 + ph * 1024);
    }
  }
  const int row = rg * 64 + mt * 16 + c;
  int colo[3];
  u32x2 xv[3], wv[3];
#pragma unroll
  for (int u = 0; u < 3; ++u) {
    colo[u] = (u == 2 ? 2 : u + 3 * kh) * 16 + 4 * g;
    xv[u] = *(const u32x2*)(p.x + (size_t)row * D + hg * 80 + colo[u]);
    wv[u] = *(const u32x2*)(p.wr + hg * 80 + colo[u]);
  }
  float sw = 0.f;
#pragma unroll
  for (int i = 0; i < 5; ++i) sw = dot2(((const unsigned*)p.wr)[lane + 64 * i], 0x3f803f80u, sw);
  for (unsigned spins = 0; spins < SPIN_CAP; ++spins) {
    const u64 f = ld_g64(ws + WS_F2 + rg * 1024 + hg * 128 + (kh * 4 + (lane & 3)) * 8);
    if (__all(f != INVALID)) break;
    __builtin_amdgcn_s_sleep(PSLEEP);
  }
  SBAR();
  u32x4 of[10];
  {
    const __amdgpu_buffer_rsrc_t rs = __builtin_amdgcn_make_buffer_rsrc((void*)(ws + WS_O + (size_t)rg * 81920), 0, 81920, 0x00020000);
#pragma unroll
    for (int s = 0; s < 10; ++s) of[s] = __builtin_amdgcn_raw_buffer_load_b128(rs, (mt * 20 + kh * 10) * 1024 + lane * 16 + s * 1024, 0, 16);
  }
  SBAR();
#pragma unroll
  for (int u = 0; u < 5; ++u) acc[u] = (f32x4){};
  PSTEPS(L_WOA, of, 0, false, false);
  BAR_LGKM();
  PSTEPS(L_WOB, of, 5, false, false);
  unsigned hw[3][2];
  {
    *(LAS f32x4*)(exw + L_EX + 3072 * (1 - kh)) = acc[3]; *(LAS f32x4*)(exw + L_EX + 3072 * (1 - kh) + 1024) = acc[4];
    if (!keep2) *(LAS f32x4*)(exw + L_EX + 2048) = acc[2];
    BAR_LGKM();
    float s1 = 0.f, dt = 0.f, sq = 0.f;
#pragma unroll
    for (int u = 0; u < 3; ++u) {
      const f32x4 a = acc[u] + *(const LAS f32x4*)(exw + L_EX + (u == 2 ? 2 : u + 3 * kh) * 1024);
      const unsigned a01 = cvtpk(a[0], a[1]), a23 = cvtpk(a[2], a[3]);
      hw[u][0] = cvtpk(bflo(xv[u][0]) + bflo(a01), bfhi(xv[u][0]) + bfhi(a01));
      hw[u][1] = cvtpk(bflo(xv[u][1]) + bflo(a23), bfhi(xv[u][1]) + bfhi(a23));
      if (u == 2 && !keep2) { hw[u][0] = 0u; hw[u][1] = 0u; }
      s1 = dot2(hw[u][0], 0x3f803f80u, dot2(hw[u][1], 0x3f803f80u, s1));
      dt = dot2(hw[u][0], wv[u][0], dot2(hw[u][1], wv[u][1], dt));
      sq = dot2(hw[u][0], hw[u][0], dot2(hw[u][1], hw[u][1], sq));
    }
    s1 = add32(add16(s1)); dt = add32(add16(dt)); sq = add32(add16(sq));
    const float ml = s1 * (1.0f / 40.f);
    const float m2 = fmaxf(sq - s1 * ml, 0.f);
    LAS f32x4* st = (LAS f32x4*)(LW + L_ST2);
    if (g == 0) st[wid * 16 + c] = (f32x4){ml, m2, dt, 0.f};
    BAR_LGKM();
    if (wid == 0) {
      const f32x4 a = st[lane], bb = st[64 + lane];
      const float dm = a[0] - bb[0];
      const unsigned q0 = (__float_as_uint(0.5f * (a[0] + bb[0])) + 0x400u) >> 11, q1 = (__float_as_uint(a[1] + bb[1] + 20.f * dm * dm) + 0x400u) >> 11,
                     q2 = (__float_as_uint(a[2] + bb[2]) + 0x400u) >> 11;
      st_g64(ws + WS_ST + (size_t)(rg * 8 + hg) * 512 + lane * 8, q0 | (q1 << 21), (q1 >> 11) | (q2 << 10));
    }
  }
  sw = add32(add16(addrow(sw))); PIN(sw);
  {
    const char* sp = ws + WS_ST + (size_t)(rg * 8 + wid) * 512 + lane * 8;
    u64 a;
    for (unsigned spins = 0; spins < SPIN_CAP; ++spins) {
      a = ld_g64(sp);
      if (__all(a != INVALID)) break;
      __builtin_amdgcn_s_sleep(PSLEEP);
    }
    LAS float* gs = (LAS float*)(LW + L_GS) + wid * 192 + lane;
    gs[0] = __uint_as_float((unsigned)a << 11); gs[64] = __uint_as_float(((unsigned)(a >> 21)) << 11); gs[128] = __uint_as_float(((unsigned)(a >> 42)) << 11);
  }
  BAR_LGKM();
  float mean_l, rstd_l, r_l; int best;
  {
    const LAS float* gs = (const LAS float*)(L + L_GS) + lane;
    float mu[8], mean = 0.f, M2 = 0.f, dt = 0.f;
#pragma unroll
    for (int i = 0; i < 8; ++i) { mu[i] = gs[i * 192]; mean += mu[i]; M2 += gs[i * 192 + 64]; dt += gs[i * 192 + 128]; }
    mean *= 0.125f;
#pragma unroll
    for (int i = 0; i < 8; ++i) { const float d = mu[i] - mean; M2 += 80.f * d * d; }
    mean_l = mean; rstd_l = __builtin_amdgcn_rsqf(M2 * (1.0f / D) + LN_EPS);
    r_l = rstd_l * (dt - mean * sw);
    const unsigned key = umax32(umax16(umaxrow((ordkey(r_l) & ~63u) | (unsigned)(63 - lane))));
    best = 63 - (int)(key & 63u);
  }
  if (hg == 0 && wid == 0) {
    const unsigned ck = (__builtin_amdgcn_readlane(ordkey(r_l), best) & ~2047u) | (unsigned)(2047 - (rg * 64 + best));
    if (lane < 32) st_g64(ws + WS_CAND + lane * 256 + rg * 8, ck, 0u);
  }
  const int lrow = mt * 16 + c;
  const float mean = __shfl(mean_l, lrow), rstd = __shfl(rstd_l, lrow);
  unsigned ow[3][2];
#pragma unroll
  for (int u = 0; u < 3; ++u) {
    const float h0 = bflo(hw[u][0]), h1 = bfhi(hw[u][0]), h2 = bflo(hw[u][1]), h3 = bfhi(hw[u][1]);
    const unsigned n01 = cvtpk((h0 - mean) * rstd, (h1 - mean) * rstd), n23 = cvtpk((h2 - mean) * rstd, (h3 - mean) * rstd);
    ow[u][0] = cvtpk(h0 + bflo(n01), h1 + bfhi(n01)); ow[u][1] = cvtpk(h2 + bflo(n23), h3 + bfhi(n23));
    if ((u < 2 || keep2) && lrow == best) {
      char* rw = ws + WS_ROW + rg * 2560 + (hg * 80 + colo[u]) * 2;
      st_val(rw, n01, n23); st_val(rw + 1280, hw[u][0], hw[u][1]);
    }
  }
  if (wid == 0) {
    u64 a;
    for (unsigned spins = 0; spins < SPIN_CAP; ++spins) {
      a = ld_g64(ws + WS_CAND + (b & 31) * 256 + r * 8);
      if (__all(a != INVALID)) break;
      __builtin_amdgcn_s_sleep(PSLEEP);
    }
    const unsigned key = umax16(umaxrow((unsigned)a));
    if (lane == 0) *(LAS unsigned*)(LW + L_RED + 1024) = key;
  }
  if (lrow != best) {
#pragma unroll
    for (int u = 0; u < 3; ++u)
      if (u < 2 || keep2) st_out((u32x2*)(p.out + (size_t)row * D + hg * 80 + colo[u]), (u32x2){ow[u][0], ow[u][1]});
  }
  BAR_LGKM();
  const unsigned win = *(const LAS unsigned*)(L + L_RED + 1024);
  const unsigned wk_ = (win & ~2047u) | 1024u;
  const float r_sel = __uint_as_float((wk_ & 0x80000000u) ? (wk_ & 0x7fffffffu) : ~wk_);
  const int widx = 2047 - (int)(win & 2047u), wrg = widx >> 6;
  if (wrg != rg && lrow == best) {
#pragma unroll
    for (int u = 0; u < 3; ++u)
      if (u < 2 || keep2) st_out((u32x2*)(p.out + (size_t)row * D + hg * 80 + colo[u]), (u32x2){ow[u][0], ow[u][1]});
  }
  if (hasG) {
    const char* rw = ws + WS_ROW + wrg * 2560;
    const int k0 = tid, k1 = tid < 128 ? tid + 512 : tid - 128, k2 = tid + 384 < 640 ? tid + 384 : 639;
    u32x4 fw[3];
#pragma unroll
    for (int j = 0; j < 3; ++j) { int e = tid + 512 * j; e = e < 1280 ? e : 1279; fw[j] = *(const LAS u32x4*)(L + L_FGU + e * 16); }
    u64 a0, a1, a2;
    for (unsigned spins = 0; spins < SPIN_CAP; ++spins) {
      a0 = ld_g64(rw + (k0 >> 2) * 8); a1 = ld_g64(rw + (k1 >> 2) * 8); a2 = ld_g64(rw + (k2 >> 2) * 8);
      if (__all((a0 != INVALID) & (a1 != INVALID) & (a2 != INVALID))) break;
      __builtin_amdgcn_s_sleep(PSLEEP);
    }
    const int sh = (tid & 3) * 16;
    const float x0 = bflo((unsigned)(a0 >> sh)), x1 = bflo((unsigned)(a1 >> sh)), x2 = tid < 256 ? bflo((unsigned)(a2 >> sh)) : 0.f;
    const float xg1 = tid < 128 ? x1 : 0.f, xu1 = tid < 128 ? 0.f : x1;
    float v8[8], v4[4];
#pragma unroll
    for (int j = 0; j < 4; ++j) {
      v8[2 * j] = rs32(x0 * bflo(fw[0][j]) + xg1 * bflo(fw[1][j]), xu1 * bflo(fw[1][j]) + x2 * bflo(fw[2][j]));
      v8[2 * j + 1] = rs32(x0 * bfhi(fw[0][j]) + xg1 * bfhi(fw[1][j]), xu1 * bfhi(fw[1][j]) + x2 * bfhi(fw[2][j]));
    }
#pragma unroll
    for (int j = 0; j < 4; ++j) v4[j] = addrow(rs16(v8[j], v8[4 + j]));
    LAS f32x4* red = (LAS f32x4*)(LW + L_RED);
    if (c == 0) red[wid * 4 + g] = (f32x4){v4[0], v4[1], v4[2], v4[3]};
    BAR_LGKM();
    if (wid == 0) {
      const LAS float* rf = (const LAS float*)(L + L_RED) + (lane & 7);
      float gv = 0.f, uv = 0.f;
#pragma unroll
      for (int w = 0; w < 8; ++w) { gv += rf[w * 16]; uv += rf[w * 16 + 8]; }
      gv = bfr(gv); uv = bfr(uv);
      const float act = bfr(gv * __builtin_amdgcn_rcpf(1.0f + __expf(-gv))) * uv;
      const unsigned pk = cvtpk(act, dppf<0xB1>(act));
      const unsigned pk2 = dppu<0x4E>(pk);
      if ((lane & 3) == 0) st_val(ws + WS_ACT + (lane >> 3) * 4096 + gsl * 16 + (lane & 4) * 2, pk, pk2);
    }
  }
  if (hasD) {
    const int tq = tid < 432 ? tid : 431;
    const int colb = dsl * 8;
    u32x4 fw[4];
#pragma unroll
    for (int j = 0; j < 4; ++j) fw[j] = *(const LAS u32x4*)(L + L_FD + (4 * tq + j) * 16);
    u64 a0, hn4, h4;
    if (!hasG) __builtin_amdgcn_s_sleep(ADELAY);
    for (unsigned spins = 0; spins < SPIN_CAP; ++spins) {
      a0 = ld_g64(ws + WS_ACT + xcd * 4096 + tq * 8);
      hn4 = ld_g64(ws + WS_ROW + wrg * 2560 + colb * 2 + (lane & 4) * 2); h4 = ld_g64(ws + WS_ROW + wrg * 2560 + 1280 + colb * 2 + (lane & 4) * 2);
      if (__all((a0 != INVALID) & (hn4 != INVALID) & (h4 != INVALID))) break;
      __builtin_amdgcn_s_sleep(ASLEEP);
    }
    float av[4] = {bflo((unsigned)a0), bfhi((unsigned)a0), bflo((unsigned)(a0 >> 32)), bfhi((unsigned)(a0 >> 32))};
    if (tid >= 432) { av[0] = av[1] = av[2] = av[3] = 0.f; }
    float y[8], v4[4], v2[2];
#pragma unroll
    for (int j = 0; j < 4; ++j) {
      y[2 * j] = (av[0] * bflo(fw[0][j]) + av[1] * bflo(fw[1][j])) + (av[2] * bflo(fw[2][j]) + av[3] * bflo(fw[3][j]));
      y[2 * j + 1] = (av[0] * bfhi(fw[0][j]) + av[1] * bfhi(fw[1][j])) + (av[2] * bfhi(fw[2][j]) + av[3] * bfhi(fw[3][j]));
    }
#pragma unroll
    for (int j = 0; j < 4; ++j) v4[j] = rs32(y[j], y[4 + j]);
#pragma unroll
    for (int j = 0; j < 2; ++j) v2[j] = addrow(rs16(v4[j], v4[2 + j]));
    LAS f32x2* red = (LAS f32x2*)(LW + L_RED + 512);
    if (c == 0) red[wid * 4 + g] = (f32x2){v2[0], v2[1]};
    BAR_LGKM();
    if (wid == 0 && lane < 8) {
      const LAS float* rf = (const LAS float*)(L + L_RED + 512) + lane;
      float tot = 0.f;
#pragma unroll
      for (int w = 0; w < 8; ++w) tot += rf[w * 8];
      const float rs = bfr(r_sel);
      const float sg = bfr(__builtin_amdgcn_rcpf(1.0f + __expf(-rs)));
      const float yv = bfr(bfr(tot) * sg);
      const float hv = bflo((unsigned)(h4 >> ((lane & 3) * 16))), hn = bflo((unsigned)(hn4 >> ((lane & 3) * 16)));
      p.out[(size_t)widx * D + colb + lane] = (u16)(cvtpk(hv + bfr(hn + yv), 0.f) & 0xffffu);
    }
  }
  }
}

extern "C" void kernel_launch(void* const* d_in, const int* in_sizes, int n_in, void* d_out, int out_size, void* d_ws, size_t ws_size, hipStream_t stream) {
  Params p;
  p.x = (const u16*)d_in[0]; p.wq = (const u16*)d_in[1]; p.wk = (const u16*)d_in[2]; p.wv = (const u16*)d_in[3]; p.wo = (const u16*)d_in[4];
  p.wr = (const u16*)d_in[6]; p.wg = (const u16*)d_in[7]; p.wu = (const u16*)d_in[8]; p.wd = (const u16*)d_in[9];
  p.out = (u16*)d_out; p.ws = (char*)d_ws;
  (void)hipFuncSetAttribute((const void*)fused_kernel, hipFuncAttributeMaxDynamicSharedMemorySize, LDS_BYTES);
  fused_kernel<<<256, 512, LDS_BYTES, stream>>>(p);
}
```
